```python
import torch

def _softmax(x, axis=-1):
    return torch.softmax(x, dim=axis)
def _gelu(x):
    return torch.nn.functional.gelu(x, approximate="tanh")
def _silu(x):
    return torch.nn.functional.silu(x)
def _sigmoid(x):
    return torch.sigmoid(x)
def _layernorm(x, axis=-1, eps=1e-5):
    m = x.mean(dim=axis, keepdim=True)
    v = x.var(dim=axis, keepdim=True, unbiased=False)
    return (x - m) / torch.sqrt(v + eps)
def _scatter_add(tgt, idx, src, axis=0):
    return tgt.clone().index_add_(axis, idx, src)
def _segment_sum(src, seg, n):
    out = torch.zeros((n,) + src.shape[1:], dtype=src.dtype)
    return out.index_add_(0, seg, src)
def _pad(x, pw):
    flat = tuple(v for pair in reversed(pw) for v in pair)
    return torch.nn.functional.pad(x, flat)

kernel_name = 'parallel_sum__linear_kernel_hedgehog__dense_activation_silu__prompt'

def setup_inputs():

    _w = (lambda a: (lambda t: t.to(torch.bfloat16) if t.is_floating_point() else t)(torch.as_tensor(a)))
    def _A_setup():
        B, S, D, H = 1, 2048, 512, 32
        Dh = D // H
        return {
            "x": _w((lambda *s: torch.randn(*s, dtype=torch.bfloat16) * 0.05)(B, S, D)),
            "wqkv": _w((lambda *s: torch.randn(*s, dtype=torch.bfloat16) * 0.2)(D, 3 * D)),
            "w_phi": _w((lambda *s: torch.randn(*s, dtype=torch.bfloat16) * 0.2)(H, Dh, Dh) * (Dh ** -0.5)),
            "wo": _w((lambda *s: torch.randn(*s, dtype=torch.bfloat16) * 0.05)(D, D)),
        }
    _A_in = _A_setup()

    def _B_setup():
        B, S, D, F = 1, 2048, 512, 1792
        return {
            "x": _w((lambda *s: torch.randn(*s, dtype=torch.bfloat16) * 0.05)(B, S, D)),
            "w1": _w((lambda *s: torch.randn(*s, dtype=torch.bfloat16) * 0.8)(D, F) * (D ** -0.5)),
            "w2": _w((lambda *s: torch.randn(*s, dtype=torch.bfloat16) * 0.1)(F, D) * (F ** -0.5)),
        }
    _B_in = _B_setup()

    return {
        "A_x": _A_in["x"],
        "A_wqkv": _A_in["wqkv"],
        "A_w_phi": _A_in["w_phi"],
        "A_wo": _A_in["wo"],
        "B_x": _B_in["x"],
        "B_w1": _B_in["w1"],
        "B_w2": _B_in["w2"],
    }

def reference(A_x, A_wqkv, A_w_phi, A_wo, B_x, B_w1, B_w2):
    def _A_ref(x, wqkv, w_phi, wo):
        B, S, D = x.shape
        H = 32; Dh = D // H
        qkv = (lambda x, *d: x.permute(*d))((x @ wqkv).reshape(B, S, 3, H, Dh), 2, 0, 3, 1, 4)
        q, k, v = qkv[0], qkv[1], qkv[2]
        pq = torch.einsum("bhsd,hdf->bhsf", q, w_phi)
        pk = torch.einsum("bhsd,hdf->bhsf", k, w_phi)
        phi_q = torch.concatenate([_softmax(pq, axis=-1), _softmax(-pq, axis=-1)], axis=-1)
        phi_k = torch.concatenate([_softmax(pk, axis=-1), _softmax(-pk, axis=-1)], axis=-1)
        kv = torch.einsum("bhsf,bhsd->bhfd", phi_k, v)
        z  = torch.einsum("bhsf->bhf", phi_k)
        num = torch.einsum("bhqf,bhfd->bhqd", phi_q, kv)
        den = torch.einsum("bhqf,bhf->bhq", phi_q, z)[..., None] + 1e-6
        o = num / den
        return (lambda x, *d: x.permute(*d))(o, 0, 2, 1, 3).reshape(B, S, D) @ wo

    def _B_ref(x, w1, w2):
        h = torch.einsum("bsd,df->bsf", x, w1)
        h = _silu(h)
        out = torch.einsum("bsf,fd->bsd", h, w2)
        return out

    x = A_x
    xn = _layernorm(x, axis=-1)
    return x + _A_ref(xn, A_wqkv, A_w_phi, A_wo) + _B_ref(xn, B_w1, B_w2)
```

```cpp
#include <hip/hip_runtime.h>
#include <hip/hip_bf16.h>
namespace naive {
constexpr int S = 2048, D = 512, H = 32, DH = 16, F = 1792;
__device__ __forceinline__ float b2f(const __hip_bfloat16 v) { return __bfloat162float(v); }

__global__ void ln_k(const __hip_bfloat16* x, float* xn) {
  int row = blockIdx.x, t = threadIdx.x;
  float v[8], s = 0;
  for (int i = 0; i < 8; ++i) { v[i] = b2f(x[row * D + t * 8 + i]); s += v[i]; }
  for (int o = 32; o; o >>= 1) s += __shfl_xor(s, o);
  float m = s / D, q = 0;
  for (int i = 0; i < 8; ++i) { float d = v[i] - m; q += d * d; }
  for (int o = 32; o; o >>= 1) q += __shfl_xor(q, o);
  float r = rsqrtf(q / D + 1e-5f);
  for (int i = 0; i < 8; ++i) xn[row * D + t * 8 + i] = (v[i] - m) * r;
}
__global__ void gemm_k(const float* A, const __hip_bfloat16* B, float* C, int M, int N, int K, int mode) {
  __shared__ float As[16][17], Bs[16][17];
  int tx = threadIdx.x, ty = threadIdx.y, row = blockIdx.y * 16 + ty, col = blockIdx.x * 16 + tx;
  float acc = 0;
  for (int k0 = 0; k0 < K; k0 += 16) {
    As[ty][tx] = A[(size_t)row * K + k0 + tx];
    Bs[ty][tx] = b2f(B[(size_t)(k0 + ty) * N + col]);
    __syncthreads();
    for (int k = 0; k < 16; ++k) acc += As[ty][k] * Bs[k][tx];
    __syncthreads();
  }
  if (mode == 1) acc = acc / (1.f + expf(-acc));
  C[(size_t)row * N + col] = acc;
}
__global__ void phi_k(const float* qkv, const __hip_bfloat16* wphi, float* phi) {
  int idx = blockIdx.x * blockDim.x + threadIdx.x;
  if (idx >= 2 * S * H) return;
  int which = idx / (S * H), s = (idx / H) % S, h = idx % H;
  const float* src = qkv + (size_t)s * 1536 + which * 512 + h * 16;
  float p[16];
  for (int f = 0; f < 16; ++f) { float a = 0; for (int d = 0; d < 16; ++d) a += src[d] * b2f(wphi[(h * 16 + d) * 16 + f]); p[f] = a; }
  float mx = p[0], mn = p[0];
  for (int f = 1; f < 16; ++f) { mx = fmaxf(mx, p[f]); mn = fminf(mn, p[f]); }
  float sp = 0, sn = 0;
  for (int f = 0; f < 16; ++f) { sp += expf(p[f] - mx); sn += expf(mn - p[f]); }
  float* o = phi + (size_t)idx * 32;
  for (int f = 0; f < 16; ++f) { o[f] = expf(p[f] - mx) / sp; o[16 + f] = expf(mn - p[f]) / sn; }
}
__global__ void kv_k(const float* qkv, const float* phi, float* kv) {
  int h = blockIdx.x, t = threadIdx.x;
  int f = t / 17, d = t % 17;
  float a = 0;
  for (int s = 0; s < S; ++s) {
    float ph = phi[((size_t)(S + s) * H + h) * 32 + f];
    float v = d < 16 ? qkv[(size_t)s * 1536 + 1024 + h * 16 + d] : 1.f;
    a += ph * v;
  }
  kv[(h * 32 + f) * 17 + d] = a;
}
__global__ void o_k(const float* phi, const float* kv, float* o) {
  int idx = blockIdx.x * blockDim.x + threadIdx.x;
  int s = idx / 512, c = idx % 512, h = c / 16, d = c % 16;
  const float* ph = phi + ((size_t)s * H + h) * 32;
  float num = 0, den = 0;
  for (int f = 0; f < 32; ++f) { num += ph[f] * kv[(h * 32 + f) * 17 + d]; den += ph[f] * kv[(h * 32 + f) * 17 + 16]; }
  o[idx] = num / (den + 1e-6f);
}
__global__ void fin_k(const __hip_bfloat16* x, const float* a, const float* b, __hip_bfloat16* out) {
  int idx = blockIdx.x * blockDim.x + threadIdx.x;
  out[idx] = __float2bfloat16(b2f(x[idx]) + a[idx] + b[idx]);
}
inline void launch(void* const* d_in, void* d_out, void* d_ws, hipStream_t stream) {
  const __hip_bfloat16* x = (const __hip_bfloat16*)d_in[0];
  const __hip_bfloat16* wqkv = (const __hip_bfloat16*)d_in[1];
  const __hip_bfloat16* wphi = (const __hip_bfloat16*)d_in[2];
  const __hip_bfloat16* wo = (const __hip_bfloat16*)d_in[3];
  const __hip_bfloat16* w1 = (const __hip_bfloat16*)d_in[5];
  const __hip_bfloat16* w2 = (const __hip_bfloat16*)d_in[6];
  float* ws = (float*)d_ws;
  float* xn = ws; float* qkv = xn + S * D; float* phi = qkv + S * 1536; float* kv = phi + 2 * S * H * 32;
  float* o = kv + H * 32 * 17 + 64; float* hbuf = o + S * D; float* ao = hbuf + S * F; float* fo = ao + S * D;
  ln_k<<<S, 64, 0, stream>>>(x, xn);
  gemm_k<<<dim3(1536 / 16, S / 16), dim3(16, 16), 0, stream>>>(xn, wqkv, qkv, S, 1536, D, 0);
  gemm_k<<<dim3(F / 16, S / 16), dim3(16, 16), 0, stream>>>(xn, w1, hbuf, S, F, D, 1);
  phi_k<<<2 * S * H / 256, 256, 0, stream>>>(qkv, wphi, phi);
  kv_k<<<H, 544, 0, stream>>>(qkv, phi, kv);
  o_k<<<S * D / 256, 256, 0, stream>>>(phi, kv, o);
  gemm_k<<<dim3(D / 16, S / 16), dim3(16, 16), 0, stream>>>(o, wo, ao, S, D, D, 0);
  gemm_k<<<dim3(D / 16, S / 16), dim3(16, 16), 0, stream>>>(hbuf, w2, fo, S, D, F, 0);
  fin_k<<<S * D / 256, 256, 0, stream>>>(x, ao, fo, (__hip_bfloat16*)d_out);
}
}
extern "C" void kernel_launch(void* const* d_in, const int* in_sizes, int n_in, void* d_out, int out_size, void* d_ws, size_t ws_size, hipStream_t stream) {
  naive::launch(d_in, d_out, d_ws, stream);
}
```

```cpp
#include <hip/hip_runtime.h>
#include <hip/hip_bf16.h>

namespace hh {
#define LAS __attribute__((address_space(3)))
#define GAS __attribute__((address_space(1)))
#define DI __device__ __forceinline__
typedef unsigned v4u __attribute__((ext_vector_type(4)));
typedef unsigned v2u __attribute__((ext_vector_type(2)));
typedef float f32x2 __attribute__((ext_vector_type(2)));
typedef float f32x4 __attribute__((ext_vector_type(4)));
typedef float f32x16 __attribute__((ext_vector_type(16)));
typedef short s16x4 __attribute__((ext_vector_type(4)));
typedef __bf16 bf16x2 __attribute__((ext_vector_type(2)));
typedef __bf16 bf16x8 __attribute__((ext_vector_type(8)));

constexpr int SLOT = 16384, NSLOT = 9;
constexpr int MISC = SLOT * NSLOT;
constexpr int L_ST = MISC;
constexpr int L_CS = L_ST + 4096;
constexpr int L_KVA = L_CS + 2048;
constexpr int L_PHI = L_KVA + 4352;
constexpr int LDS_BYTES = 163840;
static_assert(L_PHI + 4096 <= LDS_BYTES, "lds");

constexpr size_t WS_HF = 0, WS_KF = 4096, WS_FLAG_BYTES = 8192, WS_EPOCH = 8192;
constexpr size_t WS_KVP = 16384;
constexpr size_t WS_ZP = WS_KVP + 32 * 16 * 2048;
constexpr size_t WS_ACT = WS_ZP + 32 * 16 * 128;
constexpr size_t WS_END = WS_ACT + (size_t)64 * 144 * 1024;

struct Params {
  const char* x; const char* wqkv; const char* wphi; const char* wo; const char* w1; const char* w2;
  char* out; char* ws;
};

DI f32x16 mfma32(v4u a, v4u b, f32x16 c) {
  return __builtin_amdgcn_mfma_f32_32x32x16_bf16(__builtin_bit_cast(bf16x8, a), __builtin_bit_cast(bf16x8, b), c, 0, 0, 0);
}
DI unsigned pk2(float lo, float hi) { f32x2 v = {lo, hi}; return __builtin_bit_cast(unsigned, __builtin_convertvector(v, bf16x2)); }
DI v2u trr(unsigned addr) { return __builtin_bit_cast(v2u, __builtin_amdgcn_ds_read_tr16_b64_v4i16((LAS s16x4*)(size_t)addr)); }
DI v4u trfrag(unsigned a0, unsigned a1) { v2u lo = trr(a0), hi = trr(a1); return v4u{lo.x, lo.y, hi.x, hi.y}; }
DI float dot2(unsigned a, unsigned b, float c) {
  return __builtin_amdgcn_fdot2_f32_bf16(__builtin_bit_cast(bf16x2, a), __builtin_bit_cast(bf16x2, b), c, false);
}
DI float swap32_add(float v) {
  auto r = __builtin_amdgcn_permlane32_swap(__float_as_uint(v), __float_as_uint(v), false, false);
  return __uint_as_float(r[0]) + __uint_as_float(r[1]);
}
DI float swap32_max(float v) {
  auto r = __builtin_amdgcn_permlane32_swap(__float_as_uint(v), __float_as_uint(v), false, false);
  return fmaxf(__uint_as_float(r[0]), __uint_as_float(r[1]));
}
DI void dma16(unsigned voff, const char* sbase, unsigned ldsaddr) {
  asm volatile("s_mov_b32 m0, %2\n\ts_nop 3\n\tglobal_load_lds_dwordx4 %0, %1" ::"v"(voff), "s"(sbase), "s"(ldsaddr) : "memory");
}
template <int N> DI void wait_vm() { asm volatile("s_waitcnt vmcnt(%0)" ::"i"(N) : "memory"); }
DI void bar() { asm volatile("s_waitcnt lgkmcnt(0)\n\ts_barrier" ::: "memory"); }
DI v4u gload16_sc1(unsigned voff, const char* sbase) {
  v4u r; asm volatile("s_nop 4\n\tglobal_load_dwordx4 %0, %1, %2 sc1" : "=v"(r) : "v"(voff), "s"(sbase) : "memory"); return r;
}
DI v4u gload16(unsigned voff, const char* sbase) {
  v4u r; asm volatile("s_nop 4\n\tglobal_load_dwordx4 %0, %1, %2" : "=v"(r) : "v"(voff), "s"(sbase) : "memory"); return r;
}
DI void gstore16_sc1(unsigned voff, char* sbase, v4u v) {
  asm volatile("s_nop 4\n\tglobal_store_dwordx4 %0, %1, %2 sc1\n\ts_nop 1" ::"v"(voff), "v"(v), "s"(sbase) : "memory");
}
DI void gstore16(unsigned voff, char* sbase, v4u v) {
  asm volatile("s_nop 4\n\tglobal_store_dwordx4 %0, %1, %2\n\ts_nop 1" ::"v"(voff), "v"(v), "s"(sbase) : "memory");
}
DI void gstore8_sc1(unsigned voff, char* sbase, v2u v) {
  asm volatile("s_nop 4\n\tglobal_store_dwordx2 %0, %1, %2 sc1\n\ts_nop 1" ::"v"(voff), "v"(v), "s"(sbase) : "memory");
}
#define TIE(x) asm volatile("" : "+v"(x))

constexpr float LOG2E = 1.4426950408889634f;
constexpr unsigned ONES2 = 0x3f803f80u;

DI void wait_flags(const char* p, int nl, v2u tag, int lane) {
  const unsigned voff = (unsigned)((lane < nl ? lane : 0) * 16);
  for (unsigned spin = 0;; ++spin) {
    v4u f = gload16_sc1(voff, p);
    asm volatile("s_waitcnt vmcnt(0)" : "+v"(f)::"memory");
    const bool ok = f.x == tag.x && f.y == tag.y && f.z == tag.x && f.w == tag.y;
    if (__all(ok)) break;
    if (spin > (1u << 16)) break;
    __builtin_amdgcn_s_sleep(1);
  }
}

DI void phi_softmax(const f32x16 P, v4u& pos, v4u& neg) {
  float mp = fmaxf(fmaxf(fmaxf(P[0], P[1]), fmaxf(P[2], P[3])), fmaxf(fmaxf(P[4], P[5]), fmaxf(P[6], P[7])));
  float mn = fmaxf(fmaxf(fmaxf(P[8], P[9]), fmaxf(P[10], P[11])), fmaxf(fmaxf(P[12], P[13]), fmaxf(P[14], P[15])));
  mp = swap32_max(mp); mn = swap32_max(mn);
  const float cp = -mp * LOG2E, cn = -mn * LOG2E;
  float e[16];
#pragma unroll
  for (int i = 0; i < 8; ++i) { e[i] = __builtin_amdgcn_exp2f(fmaf(P[i], LOG2E, cp)); e[8 + i] = __builtin_amdgcn_exp2f(fmaf(P[8 + i], LOG2E, cn)); }
  float sp = ((e[0] + e[1]) + (e[2] + e[3])) + ((e[4] + e[5]) + (e[6] + e[7]));
  float sn = ((e[8] + e[9]) + (e[10] + e[11])) + ((e[12] + e[13]) + (e[14] + e[15]));
  sp = swap32_add(sp); sn = swap32_add(sn);
  const float rp = __builtin_amdgcn_rcpf(sp), rn = __builtin_amdgcn_rcpf(sn);
  pos = v4u{pk2(e[0] * rp, e[1] * rp), pk2(e[2] * rp, e[3] * rp), pk2(e[4] * rp, e[5] * rp), pk2(e[6] * rp, e[7] * rp)};
  neg = v4u{pk2(e[8] * rn, e[9] * rn), pk2(e[10] * rn, e[11] * rn), pk2(e[12] * rn, e[13] * rn), pk2(e[14] * rn, e[15] * rn)};
}

__host__ __device__ constexpr int s2_item(int j) { return j < 4 ? j : j < 8 ? j + 4 : j < 12 ? j - 4 : j; }
constexpr int S2N = 18, S2D = 4;
__host__ __device__ constexpr int s2_wait(int j) {
  int n = 0;
  if (j < S2D) n += 2 * (S2D - 1 - j);
  for (int k = (j < S2D ? 0 : j - S2D + 1); k < j; ++k) n += (k + 8 < S2N ? 2 : 0) + (k + S2D < S2N ? 2 : 0);
  return n;
}

__global__ void __launch_bounds__(512, 2) fused(Params p) {
  extern __shared__ __attribute__((aligned(16))) char smem[];
  const int tid = threadIdx.x, lane = tid & 63;
  const int w = __builtin_amdgcn_readfirstlane(tid >> 6);
  const int tg = w & 3, grp = w >> 2;
  const int bid = blockIdx.x, xcd = bid & 7, idx = bid >> 3;
  const int att = idx & 1;
  const int rg = 8 * (xcd & 1) + ((idx >> 1) & 7);
  const int cg = 2 * (xcd >> 1) + (idx >> 4);
  const int rt = 4 * xcd + (idx >> 3), ct = idx & 7;
  const v2u tag = {1u, 0u};
  if ((unsigned)(size_t)smem != 0u) return;

  const int wstride = att ? 3072 : 3584;
  const unsigned wdst = (unsigned)w * 2048;
  char* act = p.ws + WS_ACT;

  {
    const unsigned voffX = (lane >> 3) * 1024 + (((lane & 7) ^ (lane >> 4)) * 16);
    const char* xbase = p.x + (size_t)(rg * 128 + 16 * w) * 1024;
#pragma unroll
    for (int it = 0; it < 8; ++it) {
      dma16(voffX, xbase + it * 128, it * SLOT + wdst);
      dma16(voffX ^ 64u, xbase + it * 128 + 8192, it * SLOT + wdst + 1024);
    }
  }
  const unsigned gW = ((lane & 7) ^ (((lane >> 4) & 1) << 2)) * 16;
  const unsigned voffW = (lane >> 3) * wstride + gW;
  const unsigned voffW2 = (lane >> 3) * 1024 + gW;
  const char* wbase = (att ? p.wqkv + 128 * cg : p.w1 + 448 * cg + ((cg & 1) ? 64 : 0)) + (size_t)(16 * w) * wstride;
  const char* w2base = p.w2 + (size_t)(448 * (w >> 1) + 16 * (w & 1)) * 1024 + 128 * ct;
  auto panel_off = [&](int pi) { return att ? (pi == 0 ? 2048 : pi == 1 ? 1024 : 0) : (pi < 3 ? 128 * pi : ((cg & 1) ? -128 : 384)); };

  if (att && w < 4) {
    const int m = lane & 31, hh = lane >> 5;
    unsigned short wp[8];
#pragma unroll
    for (int j = 0; j < 8; ++j) {
      const int d = 8 * (j >> 2) + 4 * hh + (j & 3);
      wp[j] = *(const GAS unsigned short*)(p.wphi + (((4 * cg + w) * 16 + d) * 16 + (m & 15)) * 2);
    }
    const unsigned sg = (m & 16) ? 0x80008000u : 0u;
    *(LAS v4u*)(size_t)(L_PHI + w * 1024 + lane * 16) =
        v4u{(wp[0] | ((unsigned)wp[1] << 16)) ^ sg, (wp[2] | ((unsigned)wp[3] << 16)) ^ sg, (wp[4] | ((unsigned)wp[5] << 16)) ^ sg, (wp[6] | ((unsigned)wp[7] << 16)) ^ sg};
  }

  v4u xf[32];
  {
    float s1 = 0.f, s2 = 0.f;
    const unsigned row = 32 * tg + (lane & 31), hh = lane >> 5;
    const unsigned xa = row * 128 + ((hh ^ ((row >> 1) & 7)) * 16);
#pragma unroll
    for (int e = 0; e < 8; ++e) {
      wait_vm<12>(); bar();
      {
        const char* s = wbase + panel_off(e >> 2) + (size_t)((e & 3) * 128) * wstride;
        const unsigned dst = ((e + 8) % NSLOT) * SLOT + wdst;
        dma16(voffW, s, dst); dma16(voffW, s + 8 * wstride, dst + 1024);
      }
#pragma unroll
      for (int s = 0; s < 4; ++s) xf[4 * e + s] = *(LAS v4u*)(size_t)((xa ^ (32 * s)) + e * SLOT);
      if ((e >> 2) == grp) {
#pragma unroll
        for (int s = 0; s < 4; ++s) {
          const v4u v = xf[4 * e + s];
          s1 = dot2(v.x, ONES2, s1); s1 = dot2(v.y, ONES2, s1); s1 = dot2(v.z, ONES2, s1); s1 = dot2(v.w, ONES2, s1);
          s2 = dot2(v.x, v.x, s2); s2 = dot2(v.y, v.y, s2); s2 = dot2(v.z, v.z, s2); s2 = dot2(v.w, v.w, s2);
        }
      }
    }
    *(LAS f32x2*)(size_t)(L_ST + ((grp * 2 + hh) * 128 + row) * 8) = f32x2{s1, s2};
  }
  unsigned cur = 8 * SLOT, prv = 7 * SLOT;
  float lna = 0.f, lnb = 0.f;
  v4u ph[2][2];
  ph[0][0] = ph[0][1] = ph[1][0] = ph[1][1] = v4u{0, 0, 0, 0};
  const int nblk = att ? 3 : 4;
  f32x16 accp;
#pragma unroll
  for (int i = 0; i < 16; ++i) accp[i] = 0.f;

#pragma clang loop unroll(disable)
  for (int blk = 0; blk <= nblk; ++blk) {
    if (att && blk == 3) break;
    int ln = lane; TIE(ln);
    const unsigned m = ln & 31, hh = ln >> 5, q4 = (ln & 15) >> 2;
    const bool live = blk < nblk && (att || blk < 3 || grp == 0);
    const int half = (!att && blk == 3) ? (cg & 1) : grp;
    const unsigned tra = (8 * hh + q4) * 128 + ((half ^ (q4 >> 1)) * 64) + ((ln >> 4) & 1) * 32 + (ln & 3) * 8;
    const bool epi_live = att ? blk == 1 : (blk > 0 && (blk < 4 || grp == 0));
    const int pb = blk - 1;
    const bool nw = blk + 2 < nblk;
    const char* nb = nw ? wbase + panel_off(blk + 2) : w2base + (size_t)(blk + 2 - nblk) * 8 * 32768;
    const int ustep = nw ? 128 * wstride : 32768, rstep = nw ? 8 * wstride : 8192;
    const unsigned nvoff = nw ? voffW : voffW2;
    const unsigned ca = L_CS + (((pb & 1) * 2 + grp) * 4) * 128 + 16 * hh;
    f32x16 acc;
#pragma unroll
    for (int i = 0; i < 16; ++i) acc[i] = 0.f;
    float cs = 0.f;
    float hv[8];
#pragma unroll
    for (int u = 0; u < 4; ++u) {
      if (blk < nblk) {
        if (u == 3 && att && blk == 1) wait_vm<10>();
        else if ((u == 0 || u == 2) && att && blk == 2) wait_vm<8>();
        else if (u == 3 && !att && blk == 3) wait_vm<6>();
        else wait_vm<12>();
        bar();
        if (u == 0 && att && blk == 2) {
          dma16(voffW2, w2base + 2 * 32768, (13 % NSLOT) * SLOT + wdst); dma16(voffW2, w2base + 2 * 32768 + 8192, (13 % NSLOT) * SLOT + wdst + 1024);
          dma16(voffW2, w2base + 3 * 32768, (14 % NSLOT) * SLOT + wdst); dma16(voffW2, w2base + 3 * 32768 + 8192, (14 % NSLOT) * SLOT + wdst + 1024);
        }
        if (!(u >= 2 && att && blk == 1)) {
          dma16(nvoff, nb + u * ustep, prv + wdst); dma16(nvoff, nb + u * ustep + rstep, prv + wdst + 1024);
        }
        if (u == 2 && att && blk == 2 && tid == 0) gstore8_sc1(0u, p.ws + WS_KF + cg * 128 + rg * 8, tag);
        if (u == 3 && !att && blk == 3 && tid == 0) gstore8_sc1(0u, p.ws + WS_HF + rg * 256 + cg * 8, tag);
      } else if (u == 0) bar();
      if (u == 0 && blk == 0) {
        const unsigned sa = L_ST + (32 * tg + m) * 8;
        const f32x2 a = *(LAS f32x2*)(size_t)(sa), b = *(LAS f32x2*)(size_t)(sa + 1024), c = *(LAS f32x2*)(size_t)(sa + 2048), d = *(LAS f32x2*)(size_t)(sa + 3072);
        const float mean = ((a.x + b.x) + (c.x + d.x)) * (1.f / 512.f);
        const float var = fmaxf(((a.y + b.y) + (c.y + d.y)) * (1.f / 512.f) - mean * mean, 0.f);
        lna = __builtin_amdgcn_rsqf(var + 1e-5f); lnb = -lna * mean;
      }
      if (live) {
        const unsigned base = cur + tra;
#pragma unroll
        for (int s = 0; s < 8; ++s) {
          const v4u a = trfrag(base + s * 2048, base + s * 2048 + 512);
          acc = mfma32(a, xf[8 * u + s], acc);
        }
        const unsigned cb = base + tg * 4096;
        const v4u c0 = trfrag(cb, cb + 512), c1 = trfrag(cb + 2048, cb + 2560);
        cs = dot2(c0.x, ONES2, cs); cs = dot2(c0.y, ONES2, cs); cs = dot2(c0.z, ONES2, cs); cs = dot2(c0.w, ONES2, cs);
        cs = dot2(c1.x, ONES2, cs); cs = dot2(c1.y, ONES2, cs); cs = dot2(c1.z, ONES2, cs); cs = dot2(c1.w, ONES2, cs);
      }
      if (epi_live) {
        const f32x4 c = (*(LAS f32x4*)(size_t)(ca + 32 * u) + *(LAS f32x4*)(size_t)(ca + 32 * u + 128)) + (*(LAS f32x4*)(size_t)(ca + 32 * u + 256) + *(LAS f32x4*)(size_t)(ca + 32 * u + 384));
#pragma unroll
        for (int i = 0; i < 4; ++i) hv[4 * (u & 1) + i] = fmaf(lna, accp[4 * u + i], lnb * c[i]);
        if (!att) {
#pragma unroll
          for (int i = 0; i < 4; ++i) {
            const float v = hv[4 * (u & 1) + i];
            hv[4 * (u & 1) + i] = v * __builtin_amdgcn_rcpf(1.f + __builtin_amdgcn_exp2f(-LOG2E * v));
          }
        }
        if (u & 1) {
          const v4u fr = v4u{pk2(hv[0], hv[1]), pk2(hv[2], hv[3]), pk2(hv[4], hv[5]), pk2(hv[6], hv[7])};
          if (!att) {
            const int b32 = pb < 3 ? 2 * pb + grp + (cg & 1) : ((cg & 1) ? 0 : 6);
            gstore16_sc1((unsigned)ln * 16, act + ((size_t)(rg * 4 + tg) * 144 + 14 * cg + 2 * b32 + (u >> 1)) * 1024, fr);
          } else ph[0][u >> 1] = fr;
        }
      }
      prv = cur; cur = cur == 8 * SLOT ? 0u : cur + SLOT;
    }
    if (blk == nblk) {
      break;
    }
    if (live) {
      cs = swap32_add(cs);
      if (hh == 0) *(LAS float*)(size_t)(L_CS + ((((blk & 1) * 2 + grp) * 4 + tg) * 32 + m) * 4) = cs;
    }
    accp = acc;
    if (att && blk >= 1) {
      bar();
      const unsigned tok = 32 * tg + m;
      if (blk == 1) {
        const unsigned f = (tok >> 2) & 3;
#pragma unroll
        for (int hd = 0; hd < 2; ++hd) {
          const unsigned va = (15 % NSLOT) * SLOT + (2 * grp + hd) * 4096 + tok * 32;
          *(LAS v2u*)(size_t)(va + ((hh ^ f) * 8)) = v2u{ph[0][hd].x, ph[0][hd].y};
          *(LAS v2u*)(size_t)(va + (((2 + hh) ^ f) * 8)) = v2u{ph[0][hd].z, ph[0][hd].w};
        }
      }
      const unsigned cq = L_CS + (((blk & 1) * 2 + grp) * 4) * 128 + 16 * hh;
#pragma unroll
      for (int hd = 0; hd < 2; ++hd) {
        float t[8];
#pragma unroll
        for (int u = 0; u < 2; ++u) {
          const unsigned a = cq + 32 * (2 * hd + u);
          const f32x4 c = (*(LAS f32x4*)(size_t)(a) + *(LAS f32x4*)(size_t)(a + 128)) + (*(LAS f32x4*)(size_t)(a + 256) + *(LAS f32x4*)(size_t)(a + 384));
#pragma unroll
          for (int i = 0; i < 4; ++i) t[4 * u + i] = fmaf(lna, acc[8 * hd + 4 * u + i], lnb * c[i]);
        }
        const v4u fr = v4u{pk2(t[0], t[1]), pk2(t[2], t[3]), pk2(t[4], t[5]), pk2(t[6], t[7])};
        const v4u aphi = *(LAS v4u*)(size_t)(L_PHI + (2 * grp + hd) * 1024 + ln * 16);
        f32x16 z;
#pragma unroll
        for (int i = 0; i < 16; ++i) z[i] = 0.f;
        const f32x16 P = mfma32(aphi, fr, z);
        phi_softmax(P, ph[hd][0], ph[hd][1]);
      }
      if (blk == 1) {
        const unsigned f = (tok >> 1) & 7;
#pragma unroll
        for (int hd = 0; hd < 2; ++hd) {
          const unsigned pa = (13 % NSLOT + grp) * SLOT + hd * 8192 + tok * 64;
          *(LAS v2u*)(size_t)(pa + (((0 + hh) ^ f) * 8)) = v2u{ph[hd][0].x, ph[hd][0].y};
          *(LAS v2u*)(size_t)(pa + (((2 + hh) ^ f) * 8)) = v2u{ph[hd][0].z, ph[hd][0].w};
          *(LAS v2u*)(size_t)(pa + (((4 + hh) ^ f) * 8)) = v2u{ph[hd][1].x, ph[hd][1].y};
          *(LAS v2u*)(size_t)(pa + (((6 + hh) ^ f) * 8)) = v2u{ph[hd][1].z, ph[hd][1].w};
        }
        bar();
        if (grp == 0) {
          f32x16 kv;
#pragma unroll
          for (int i = 0; i < 16; ++i) kv[i] = 0.f;
          const unsigned gi1 = (ln >> 4) & 1, p4 = ln & 3;
          const unsigned pa0 = (13 % NSLOT) * SLOT + tg * 8192 + (8 * hh + q4) * 64 + (((4 * gi1 + p4) ^ (4 * hh + (q4 >> 1))) * 8);
          const unsigned pa1 = (pa0 ^ 16u) + 256;
          const unsigned va0 = (15 % NSLOT) * SLOT + tg * 4096 + (8 * hh + q4) * 32 + ((p4 ^ (2 * hh)) * 8);
          const unsigned va1 = (va0 ^ 8u) + 128;
#pragma unroll
          for (int s = 0; s < 8; ++s) {
            const v4u a = trfrag(pa0 + s * 1024, pa1 + s * 1024);
            v4u b = trfrag(va0 + s * 512, va1 + s * 512);
            if (gi1) b = v4u{ONES2, ONES2, ONES2, ONES2};
            kv = mfma32(a, b, kv);
          }
          asm volatile("s_nop 15" : "+v"(kv));
          if (m <= 16) {
            const int head = 4 * cg + tg;
            const unsigned off = m < 16 ? (unsigned)(WS_KVP + (size_t)(head * 16 + rg) * 2048 + (hh * 16 + m) * 16)
                                        : (unsigned)(WS_ZP + (size_t)(head * 16 + rg) * 128 + hh * 16);
            const unsigned st = m < 16 ? 512u : 32u;
#pragma unroll
            for (int i = 0; i < 4; ++i)
              gstore16_sc1(off + i * st, p.ws, v4u{__float_as_uint(kv[4 * i]), __float_as_uint(kv[4 * i + 1]), __float_as_uint(kv[4 * i + 2]), __float_as_uint(kv[4 * i + 3])});
          }
        }
      }
    }
  }
  cur = att ? (20 % NSLOT) * SLOT : (24 % NSLOT) * SLOT;
  prv = att ? (19 % NSLOT) * SLOT : (23 % NSLOT) * SLOT;

  int ln = lane; TIE(ln);
  const unsigned m = ln & 31, hh = ln >> 5;
  if (att) {
    wait_flags(p.ws + WS_KF + cg * 128, 8, tag, ln);
    v4u part[16];
    const int hl = w >> 1, ch = (w & 1) * 64 + ln;
    const char* kb = p.ws + WS_KVP + (size_t)((4 * cg + hl) * 16) * 2048;
#pragma unroll
    for (int r = 0; r < 16; ++r) part[r] = gload16_sc1(ch * 16, kb + r * 2048);
    f32x4 zs = {0.f, 0.f, 0.f, 0.f};
    if (w == 0) {
      v4u zp[16];
      const unsigned zoff = (m >> 3) * 2048 + (m & 7) * 16;
      const char* zb = p.ws + WS_ZP + (size_t)(4 * cg * 16) * 128;
#pragma unroll
      for (int r = 0; r < 16; ++r) zp[r] = gload16_sc1(zoff, zb + r * 128);
      asm volatile("s_waitcnt vmcnt(0)" : "+v"(zp[0]), "+v"(zp[1]), "+v"(zp[2]), "+v"(zp[3]), "+v"(zp[4]), "+v"(zp[5]), "+v"(zp[6]), "+v"(zp[7])::"memory");
      asm volatile("" : "+v"(zp[8]), "+v"(zp[9]), "+v"(zp[10]), "+v"(zp[11]), "+v"(zp[12]), "+v"(zp[13]), "+v"(zp[14]), "+v"(zp[15]));
#pragma unroll
      for (int r = 0; r < 16; ++r) zs += __builtin_bit_cast(f32x4, zp[r]);
    }
    asm volatile("s_waitcnt vmcnt(0)" : "+v"(part[0]), "+v"(part[1]), "+v"(part[2]), "+v"(part[3]), "+v"(part[4]), "+v"(part[5]), "+v"(part[6]), "+v"(part[7])::"memory");
    asm volatile("" : "+v"(part[8]), "+v"(part[9]), "+v"(part[10]), "+v"(part[11]), "+v"(part[12]), "+v"(part[13]), "+v"(part[14]), "+v"(part[15]));
    f32x4 ks4 = __builtin_bit_cast(f32x4, part[0]);
#pragma unroll
    for (int r = 1; r < 16; ++r) ks4 += __builtin_bit_cast(f32x4, part[r]);
    {
      const int i = ch >> 5, h2 = (ch >> 4) & 1, c = ch & 15;
      *(LAS v2u*)(size_t)(L_KVA + (((hl * 2 + (i >> 1)) * 2 + h2) * 17 + c) * 16 + (i & 1) * 8) = v2u{pk2(ks4[0], ks4[1]), pk2(ks4[2], ks4[3])};
      if (w == 0 && hh == 0) {
        const int zh = m >> 3, zi = (m & 7) >> 1, z2 = m & 1;
        *(LAS v2u*)(size_t)(L_KVA + (((zh * 2 + (zi >> 1)) * 2 + z2) * 17 + 16) * 16 + (zi & 1) * 8) = v2u{pk2(zs[0], zs[1]), pk2(zs[2], zs[3])};
      }
    }
    bar();
#pragma unroll
    for (int hd = 0; hd < 2; ++hd) {
      const int hl2 = 2 * grp + hd;
      const unsigned ka = L_KVA + ((hl2 * 4 + hh) * 17 + (m < 16 ? m : 16)) * 16;
      const v4u a0 = *(LAS v4u*)(size_t)(ka), a1 = *(LAS v4u*)(size_t)(ka + 2 * 17 * 16);
      f32x16 o;
#pragma unroll
      for (int i = 0; i < 16; ++i) o[i] = 0.f;
      o = mfma32(a0, ph[hd][0], o);
      o = mfma32(a1, ph[hd][1], o);
      const float r = __builtin_amdgcn_rcpf(o[8] + 1e-6f);
      gstore16_sc1((unsigned)ln * 16, act + ((size_t)(rg * 4 + tg) * 144 + 112 + 4 * cg + hl2) * 1024,
                   v4u{pk2(o[0] * r, o[1] * r), pk2(o[2] * r, o[3] * r), pk2(o[4] * r, o[5] * r), pk2(o[6] * r, o[7] * r)});
    }
  }

  {
    const int kq = tg, th = grp;
    const char* hf = p.ws + WS_HF + (rt >> 1) * 256 + kq * 16;
    const char* abase = act + ((size_t)(2 * rt + th) * 144 + 28 * kq) * 1024;
    const char* obase = act + ((size_t)(2 * rt + th) * 144 + 112 + 8 * kq) * 1024;
    const char* wobase = p.wo + (size_t)(128 * (w >> 1) + 16 * (w & 1)) * 1024 + 128 * ct;
    const unsigned l16 = (unsigned)ln * 16;
    const unsigned t8 = (unsigned)(w * 8 + (ln >> 3)), c8 = ln & 7;
    v4u hb[S2D + 1][2];
    v4u xres = gload16(t8 * 1024 + c8 * 16, p.x + (size_t)(64 * rt) * 1024 + 128 * ct);
    wait_flags(hf, 1, tag, ln);
#pragma unroll
    for (int j = 0; j < S2D; ++j) {
      const char* s = abase + s2_item(j) * 2048;
      hb[j][0] = gload16_sc1(l16, s); hb[j][1] = gload16_sc1(l16, s + 1024);
    }
    f32x16 acc0, acc1;
#pragma unroll
    for (int i = 0; i < 16; ++i) { acc0[i] = 0.f; acc1[i] = 0.f; }
    const unsigned q4 = (ln & 15) >> 2;
    const unsigned tra2 = kq * 4096 + (4 * hh + q4) * 128 + ((q4 >> 1) * 64) + ((ln >> 4) & 1) * 32 + (ln & 3) * 8;
#pragma unroll
    for (int j = 0; j < S2N; ++j) {
      asm volatile("s_waitcnt vmcnt(%2)" : "+v"(hb[j % (S2D + 1)][0]), "+v"(hb[j % (S2D + 1)][1]) : "i"(s2_wait(j)) : "memory");
      bar();
      if (j == 0 && tid == 0) gstore8_sc1(0u, p.ws + WS_HF + rg * 256 + cg * 8 + (att ? 128 : 64), tag);
      if (j + 8 < S2N) {
        const int id = s2_item(j + 8);
        const char* s = id < 14 ? w2base + id * 32768 : wobase + (id - 14) * 32768;
        dma16(voffW2, s, prv + wdst); dma16(voffW2, s + 8192, prv + wdst + 1024);
      }
      if (j + S2D < S2N) {
        if (j + S2D == 8) wait_flags(hf + 64, 1, tag, ln);
        if (j + S2D == 14) wait_flags(hf + 128, 1, tag, ln);
        const int id = s2_item(j + S2D);
        const char* s = id < 14 ? abase + id * 2048 : obase + (id - 14) * 2048;
        hb[(j + S2D) % (S2D + 1)][0] = gload16_sc1(l16, s); hb[(j + S2D) % (S2D + 1)][1] = gload16_sc1(l16, s + 1024);
      }
      const unsigned b0 = cur + tra2, b1 = cur + (tra2 ^ 64u);
#pragma unroll
      for (int u = 0; u < 2; ++u) {
        const v4u a0 = trfrag(b0 + u * 2048, b0 + u * 2048 + 1024), a1 = trfrag(b1 + u * 2048, b1 + u * 2048 + 1024);
        acc0 = mfma32(a0, hb[j % (S2D + 1)][u], acc0);
        acc1 = mfma32(a1, hb[j % (S2D + 1)][u], acc1);
      }
      prv = cur; cur = cur == 8 * SLOT ? 0u : cur + SLOT;
    }
    bar();
    {
      const unsigned ra = kq * 17408 + (32 * th + m) * 272 + hh * 16;
#pragma unroll
      for (int i = 0; i < 4; ++i) {
        *(LAS f32x4*)(size_t)(ra + i * 32) = f32x4{acc0[4 * i], acc0[4 * i + 1], acc0[4 * i + 2], acc0[4 * i + 3]};
        *(LAS f32x4*)(size_t)(ra + 128 + i * 32) = f32x4{acc1[4 * i], acc1[4 * i + 1], acc1[4 * i + 2], acc1[4 * i + 3]};
      }
    }
    bar();
    {
      const unsigned ra = t8 * 272 + c8 * 32;
      f32x4 lo = (*(LAS f32x4*)(size_t)(ra) + *(LAS f32x4*)(size_t)(ra + 17408)) + (*(LAS f32x4*)(size_t)(ra + 2 * 17408) + *(LAS f32x4*)(size_t)(ra + 3 * 17408));
      f32x4 hi = (*(LAS f32x4*)(size_t)(ra + 16) + *(LAS f32x4*)(size_t)(ra + 17408 + 16)) + (*(LAS f32x4*)(size_t)(ra + 2 * 17408 + 16) + *(LAS f32x4*)(size_t)(ra + 3 * 17408 + 16));
      TIE(xres);
      const v4u o = v4u{pk2(lo[0] + __uint_as_float(xres.x << 16), lo[1] + __uint_as_float(xres.x & 0xffff0000u)),
                        pk2(lo[2] + __uint_as_float(xres.y << 16), lo[3] + __uint_as_float(xres.y & 0xffff0000u)),
                        pk2(hi[0] + __uint_as_float(xres.z << 16), hi[1] + __uint_as_float(xres.z & 0xffff0000u)),
                        pk2(hi[2] + __uint_as_float(xres.w << 16), hi[3] + __uint_as_float(xres.w & 0xffff0000u))};
      gstore16(t8 * 1024 + c8 * 16, p.out + (size_t)(64 * rt) * 1024 + 128 * ct, o);
    }
  }
}
}

extern "C" void kernel_launch(void* const* d_in, const int* in_sizes, int n_in, void* d_out, int out_size, void* d_ws, size_t ws_size,
                              hipStream_t stream) {
  hh::Params p;
  p.x = (const char*)d_in[0]; p.wqkv = (const char*)d_in[1]; p.wphi = (const char*)d_in[2]; p.wo = (const char*)d_in[3];
  p.w1 = (const char*)d_in[5]; p.w2 = (const char*)d_in[6]; p.out = (char*)d_out; p.ws = (char*)d_ws;
  (void)hipFuncSetAttribute((const void*)hh::fused, hipFuncAttributeMaxDynamicSharedMemorySize, hh::LDS_BYTES);
  (void)hipMemsetAsync(d_ws, 0, hh::WS_FLAG_BYTES, stream);
  hh::fused<<<256, 512, hh::LDS_BYTES, stream>>>(p);
}
```

```cpp
#include <hip/hip_runtime.h>
#include <hip/hip_bf16.h>

namespace hh {
#define LAS __attribute__((address_space(3)))
#define GAS __attribute__((address_space(1)))
#define DI __device__ __forceinline__
typedef unsigned v4u __attribute__((ext_vector_type(4)));
typedef unsigned v2u __attribute__((ext_vector_type(2)));
typedef float f32x2 __attribute__((ext_vector_type(2)));
typedef float f32x4 __attribute__((ext_vector_type(4)));
typedef float f32x16 __attribute__((ext_vector_type(16)));
typedef short s16x4 __attribute__((ext_vector_type(4)));
typedef __bf16 bf16x2 __attribute__((ext_vector_type(2)));
typedef __bf16 bf16x8 __attribute__((ext_vector_type(8)));

constexpr int SLOT = 16384, NSLOT = 9;
constexpr int MISC = SLOT * NSLOT;
constexpr int L_ST = MISC;
constexpr int L_CS = L_ST + 2048;
constexpr int L_KVA = L_CS + 4096;
constexpr int L_PHI = L_KVA + 4352;
constexpr int L_MBH = L_PHI + 2048;
constexpr int L_MBK = L_MBH + 256;
constexpr int LDS_BYTES = 163840;
static_assert(L_MBK + 128 <= LDS_BYTES, "lds");

constexpr size_t WS_HF = 0, WS_KF = 4096, WS_EPOCH = 8192;
constexpr size_t WS_KVP = 16384;
constexpr size_t WS_ZP = WS_KVP + 32 * 16 * 2048;
constexpr size_t WS_ACT = WS_ZP + 32 * 16 * 128;
constexpr size_t WS_END = WS_ACT + (size_t)64 * 144 * 1024;
constexpr size_t WS_STAMP = 64u << 20;
#define STAMP(k)
#define CSTAMP(k)
struct Params {
  const char* x; const char* wqkv; const char* wphi; const char* wo; const char* w1; const char* w2;
  char* out; char* ws;
};

DI f32x16 mfma32(v4u a, v4u b, f32x16 c) {
  return __builtin_amdgcn_mfma_f32_32x32x16_bf16(__builtin_bit_cast(bf16x8, a), __builtin_bit_cast(bf16x8, b), c, 0, 0, 0);
}
DI f32x4 mfma16(v4u a, v4u b, f32x4 c) {
  return __builtin_amdgcn_mfma_f32_16x16x32_bf16(__builtin_bit_cast(bf16x8, a), __builtin_bit_cast(bf16x8, b), c, 0, 0, 0);
}
DI f32x16 zero16() { f32x16 z;
#pragma unroll
  for (int i = 0; i < 16; ++i) z[i] = 0.f;
  return z; }
DI unsigned pk2(float lo, float hi) { f32x2 v = {lo, hi}; return __builtin_bit_cast(unsigned, __builtin_convertvector(v, bf16x2)); }
DI unsigned pk2v(f32x2 v) { return __builtin_bit_cast(unsigned, __builtin_convertvector(v, bf16x2)); }
DI v2u trr(unsigned addr) { return __builtin_bit_cast(v2u, __builtin_amdgcn_ds_read_tr16_b64_v4i16((LAS s16x4*)(size_t)addr)); }
DI v4u trfrag(unsigned a0, unsigned a1) { v2u lo = trr(a0), hi = trr(a1); return v4u{lo.x, lo.y, hi.x, hi.y}; }
DI float dot2(unsigned a, unsigned b, float c) {
  return __builtin_amdgcn_fdot2_f32_bf16(__builtin_bit_cast(bf16x2, a), __builtin_bit_cast(bf16x2, b), c, false);
}
extern "C" __device__ float hh_fmul_legacy(float, float) __asm("llvm.amdgcn.fmul.legacy");
DI float fmul(float a, float b) { return hh_fmul_legacy(a, b); }
DI float lselm(float a, float b, unsigned long long mask) { float r; asm("v_cndmask_b32 %0, %1, %2, %3" : "=v"(r) : "v"(a), "v"(b), "s"(mask)); return r; }
template <unsigned B> DI float lsel(float a, float b) { return lselm(a, b, 0x0101010101010101ull * B); }
DI float swap32_add(float v) {
  auto r = __builtin_amdgcn_permlane32_swap(__float_as_uint(v), __float_as_uint(v), false, false);
  return __uint_as_float(r[0]) + __uint_as_float(r[1]);
}
DI float swap32_max(float v) {
  auto r = __builtin_amdgcn_permlane32_swap(__float_as_uint(v), __float_as_uint(v), false, false);
  return fmaxf(__uint_as_float(r[0]), __uint_as_float(r[1]));
}
typedef __amdgpu_buffer_rsrc_t rsrc_t;
DI rsrc_t make_rsrc(const char* base, unsigned bytes) { return __builtin_amdgcn_make_buffer_rsrc((void*)base, 0, bytes, 0x00020000); }
template <unsigned LO, unsigned GO> DI void dma2(unsigned v0, unsigned v1, rsrc_t rs, unsigned sc, unsigned ld) {
  unsigned t;
  asm volatile("s_add_u32 m0, %5, %6\n\ts_add_u32 %0, %4, %7\n\tbuffer_load_dwordx4 %1, %3, %0 offen lds\n\tbuffer_load_dwordx4 %2, %3, %0 offen offset:1024 lds"
               : "=&s"(t) : "v"(v0), "v"(v1), "s"(rs), "s"(sc), "s"(ld), "i"(LO), "i"(GO) : "memory", "scc");
}
DI void dma1_sc1(unsigned v0, const char* s0, unsigned dst) {
  asm volatile("s_mov_b32 m0, %2\n\ts_nop 3\n\tglobal_load_lds_dwordx4 %0, %1 sc1" ::"v"(v0), "s"(s0), "s"(dst) : "memory");
}
DI void dma1_sc1_m(unsigned v0, const char* s0, unsigned dst, unsigned long long mask) {
  asm volatile("s_mov_b32 m0, %2\n\ts_mov_b64 exec, %3\n\tglobal_load_lds_dwordx4 %0, %1 sc1\n\ts_mov_b64 exec, -1" ::"v"(v0), "s"(s0), "s"(dst), "s"(mask) : "memory");
}
DI void flag_store_m(unsigned zero, char* sbase, v2u v, unsigned long long mask) {
  asm volatile("s_mov_b64 exec, %3\n\tglobal_store_dwordx2 %0, %1, %2 sc1\n\ts_mov_b64 exec, -1\n\ts_nop 0" ::"v"(zero), "v"(v), "s"(sbase), "s"(mask) : "memory");
}
template <int N> DI void sync() { asm volatile("s_waitcnt vmcnt(%0) lgkmcnt(0)\n\ts_barrier" ::"i"(N) : "memory"); }
DI void bar() { asm volatile("s_waitcnt lgkmcnt(0)\n\ts_barrier" ::: "memory"); }
DI v4u gload16_sc1(unsigned voff, const char* sbase) {
  v4u r; asm volatile("s_nop 4\n\tglobal_load_dwordx4 %0, %1, %2 sc1" : "=v"(r) : "v"(voff), "s"(sbase) : "memory"); return r;
}
template <unsigned GO> DI void bload16x2_sc1(v4u& a, v4u& b, unsigned voff, rsrc_t rs, unsigned sc) {
  unsigned t;
  asm volatile("s_add_u32 %2, %5, %6\n\tbuffer_load_dwordx4 %0, %3, %4, %2 offen sc1\n\tbuffer_load_dwordx4 %1, %3, %4, %2 offen offset:1024 sc1"
               : "=&v"(a), "=&v"(b), "=&s"(t) : "v"(voff), "s"(rs), "s"(sc), "i"(GO) : "memory", "scc");
}
template <unsigned GO> DI void bload16x2s_sc1(v4u& a, v4u& b, unsigned voff, rsrc_t rs, unsigned sc) {
  unsigned t;
  asm volatile("s_add_u32 %2, %5, %6\n\tbuffer_load_dwordx4 %0, %3, %4, %2 offen sc1\n\tbuffer_load_dwordx4 %1, %3, %4, %2 offen offset:2048 sc1"
               : "=&v"(a), "=&v"(b), "=&s"(t) : "v"(voff), "s"(rs), "s"(sc), "i"(GO) : "memory", "scc");
}
template <unsigned GO> DI void bstore16_sc1(v4u v, unsigned voff, rsrc_t rs, unsigned sc) {
  unsigned t;
  asm volatile("s_add_u32 %0, %4, %5\n\tbuffer_store_dwordx4 %1, %2, %3, %0 offen sc1\n\ts_nop 1" : "=&s"(t) : "v"(v), "v"(voff), "s"(rs), "s"(sc), "i"(GO) : "memory", "scc");
}
DI v4u gload16(unsigned voff, const char* sbase) {
  v4u r; asm volatile("s_nop 4\n\tglobal_load_dwordx4 %0, %1, %2" : "=v"(r) : "v"(voff), "s"(sbase) : "memory"); return r;
}
DI void gstore16_sc1(unsigned voff, char* sbase, v4u v) {
  asm volatile("s_nop 4\n\tglobal_store_dwordx4 %0, %1, %2 sc1\n\ts_nop 1" ::"v"(voff), "v"(v), "s"(sbase) : "memory");
}
DI void gstore16(unsigned voff, char* sbase, v4u v) {
  asm volatile("s_nop 4\n\tglobal_store_dwordx4 %0, %1, %2\n\ts_nop 1" ::"v"(voff), "v"(v), "s"(sbase) : "memory");
}
DI void gstore8_sc1(unsigned voff, char* sbase, v2u v) {
  asm volatile("s_nop 4\n\tglobal_store_dwordx2 %0, %1, %2 sc1\n\ts_nop 1" ::"v"(voff), "v"(v), "s"(sbase) : "memory");
}
#define TIE(x) asm volatile("" : "+v"(x))
#define SB() __builtin_amdgcn_sched_barrier(0)

constexpr float LOG2E = 1.4426950408889634f;
constexpr unsigned ONES2 = 0x3f803f80u;
constexpr unsigned SPIN_CAP = 1u << 16;

template <int N, class F> DI void static_for(F&& f) {
  if constexpr (N > 0) { static_for<N - 1>(f); f(std::integral_constant<int, N - 1>{}); }
}

DI void wait_mailbox(unsigned mb, int nl, const char* src, v2u tag, int lane, int w) {
  const unsigned a = mb + (unsigned)((lane < nl ? lane : 0) * 16);
  for (unsigned spin = 0;; ++spin) {
    const v4u f = *(volatile LAS v4u*)(size_t)a;
    const bool ok = f.x == tag.x && f.y == tag.y && f.z == tag.x && f.w == tag.y;
    if (__all(ok)) break;
    if (spin > SPIN_CAP) break;
    if (w == 0) {
      if (lane < nl) dma1_sc1((unsigned)lane * 16, src, mb);
      asm volatile("s_waitcnt vmcnt(0)" ::: "memory");
    } else __builtin_amdgcn_s_sleep(1);
  }
  asm volatile("" ::: "memory");
}

DI float max3(float a, float b, float c) { float r; asm("v_max3_f32 %0, %1, %2, %3" : "=v"(r) : "v"(a), "v"(b), "v"(c)); return r; }
DI float max2(float a, float b) { float r; asm("v_max_f32 %0, %1, %2" : "=v"(r) : "v"(a), "v"(b)); return r; }
DI float max8(const f32x16& P, int o) { return max2(max3(P[o], P[o + 1], P[o + 2]), max3(P[o + 3], P[o + 4], max3(P[o + 5], P[o + 6], P[o + 7]))); }
template <bool MAX> DI void cross2(float& a, float& b) {
  auto r = __builtin_amdgcn_permlane32_swap(__float_as_uint(a), __float_as_uint(b), false, false);
  const float t = MAX ? max2(__uint_as_float(r[0]), __uint_as_float(r[1])) : __uint_as_float(r[0]) + __uint_as_float(r[1]);
  unsigned t2 = __float_as_uint(t); asm volatile("s_nop 1" : "+v"(t2));
  auto s = __builtin_amdgcn_permlane32_swap(t2, t2, false, false);
  a = __uint_as_float(s[0]); b = __uint_as_float(s[1]);
}
DI void phi_softmax(const f32x16& P, const float sc, v4u& pos, v4u& neg) {
  float mp = max8(P, 0), mn = max8(P, 8);
  asm volatile("s_nop 1" : "+v"(mp), "+v"(mn));
  cross2<true>(mp, mn);
  const f32x2 s2 = {sc, sc}, cp = {-mp * sc, -mp * sc}, cn = {-mn * sc, -mn * sc};
  f32x2 e[8];
#pragma unroll
  for (int i = 0; i < 8; ++i) {
    const f32x2 a = __builtin_elementwise_fma(f32x2{P[2 * i], P[2 * i + 1]}, s2, i < 4 ? cp : cn);
    e[i] = f32x2{__builtin_amdgcn_exp2f(a.x), __builtin_amdgcn_exp2f(a.y)};
  }
  const f32x2 p2 = (e[0] + e[1]) + (e[2] + e[3]), n2 = (e[4] + e[5]) + (e[6] + e[7]);
  float sp = p2.x + p2.y, sn = n2.x + n2.y;
  cross2<false>(sp, sn);
  const float rp = __builtin_amdgcn_rcpf(sp), rn = __builtin_amdgcn_rcpf(sn);
  const f32x2 rp2 = {rp, rp}, rn2 = {rn, rn};
  pos = v4u{pk2v(e[0] * rp2), pk2v(e[1] * rp2), pk2v(e[2] * rp2), pk2v(e[3] * rp2)};
  neg = v4u{pk2v(e[4] * rn2), pk2v(e[5] * rn2), pk2v(e[6] * rn2), pk2v(e[7] * rn2)};
}

constexpr bool it_isx(int i) { return i < 12 && (i % 3) != 2; }
constexpr int it_x(int i) { return 2 * (i / 3) + (i % 3); }
constexpr int it_p(int i) { return i < 12 ? 0 : (i - 12) / 4 + 1; }
constexpr int it_q(int i) { return i < 12 ? i / 3 : (i - 12) % 4; }
constexpr int n1_of(bool att) { return att ? 20 : 24; }
constexpr unsigned slot_of(int i) { return (unsigned)(i % NSLOT) * SLOT; }
constexpr int s2_id(int j) { return j < 12 ? 2 * (j / 4) + (j & 1) + ((j & 2) ? 8 : 0) : j < 14 ? j - 6 : j; }
constexpr int S2N = 18;
constexpr int issued_before(bool att, int i) { return i == 12 ? 17 : (att && i >= 15 && i <= 18) ? 21 : (att && i == 19) ? 24 : i + 7; }
constexpr int S2_B = 0, S2_C = 3, S2_O = 9;
constexpr int s2_ld(int j) { return j < 4 ? -1 : j < 8 ? S2_B : j < 14 ? S2_C + (j - 8) / 3 : S2_O; }
constexpr int s2_cnt(int k) { int n = 0; for (int j = 0; j < S2N; ++j) n += s2_ld(j) == k; return n; }
constexpr int s2_first(int k) { for (int j = 0; j < S2N; ++j) if (s2_ld(j) == k) return j; return 0; }
constexpr int s2_ops(int k) {
  int n = (k >= 0 && k + 9 < S2N) ? 2 : 0;
  for (int j = 0; j < S2N; ++j) n += s2_ld(j) == k ? 2 : 0;
  return n;
}
constexpr int s2_since(int k0, int k1) { int n = 0; for (int k = k0; k < k1; ++k) n += s2_ops(k); return n; }
constexpr int s2_wait(int j) {
  int n = s2_since(s2_ld(j) + 1, j);
  for (int t = j + 1; t < S2N; ++t) n += s2_ld(t) == s2_ld(j) ? 2 : 0;
  if (j + 1 >= 9 && j + 1 < S2N) { const int nd = s2_since(j - 8, j) - 2; if (nd < n) n = nd; }
  return n;
}
constexpr int S2_LASTBAR = 14;
static_assert(S2_O >= 8, "S2_LASTBAR");
template <int V> using IC = std::integral_constant<int, V>;

struct Ctx {
  int lane, w, tg, grp, rg, cg, rt, ct;
  unsigned wdst, vW2a, vW2b, s2off;
  unsigned s2offo;
  rsrc_t rW2, rWo, rWs;
  v2u tag;
  unsigned long long m16, m1;
  float lna, lni, lnz;
};

template <int J, unsigned LO> DI void issue_s2(const Ctx& c, unsigned ld) {
  constexpr int id = s2_id(J);
  if constexpr (id < 14) dma2<LO, id * 32768u>(c.vW2a, c.vW2b, c.rW2, c.s2off, ld);
  else dma2<LO, (id - 14) * 32768u>(c.vW2a, c.vW2b, c.rWo, c.s2offo, ld);
}
DI float silu_a(float a, const Ctx& c) { return __builtin_amdgcn_exp2f(fmul(a, c.lnz)); }
DI float silu_b(float a, float t, const Ctx& c) { return fmul(a, __builtin_amdgcn_rcpf(fmaf(t, c.lni, c.lni))); }
template <class F> DI void phi_block(const f32x16& A, const Ctx& c, v4u (&ph)[2][2], F&& between) {
  const unsigned pa = L_PHI + c.grp * 1024 + (4 * (c.lane >> 5) + ((c.lane & 15) >> 2)) * 32 + (c.lane & 3) * 8;
  const unsigned sg = (c.lane & 16) ? 0x80008000u : 0u;
  f32x16 P[2];
#pragma unroll
  for (int hd = 0; hd < 2; ++hd) {
    const v4u fr = v4u{pk2(A[8 * hd], A[8 * hd + 1]), pk2(A[8 * hd + 2], A[8 * hd + 3]), pk2(A[8 * hd + 4], A[8 * hd + 5]), pk2(A[8 * hd + 6], A[8 * hd + 7])};
    const v4u aphi = trfrag(pa + hd * 512, pa + hd * 512 + 256) ^ sg;
    P[hd] = mfma32(aphi, fr, zero16());
  }
  between(IC<0>{});
  asm volatile("s_nop 15" : "+v"(P[0]), "+v"(P[1]));
  phi_softmax(P[0], -c.lnz, ph[0][0], ph[0][1]);
  between(IC<1>{});
  phi_softmax(P[1], -c.lnz, ph[1][0], ph[1][1]);
}

template <bool ATT>
DI void stage1(const Params& p, Ctx& c, v4u (&ph)[2][2], v4u (&a2)[4], f32x16& last, const unsigned tra2) {
  constexpr int N1 = n1_of(ATT);
  constexpr unsigned WS_ = ATT ? 3072 : 3584;
  const int lane = c.lane, w = c.w, tg = c.tg, grp = c.grp, rg = c.rg, cg = c.cg;
  const unsigned m = lane & 31, hh = lane >> 5, q4 = (lane & 15) >> 2;
  const unsigned vXa = (lane >> 3) * 1024 + (((lane & 7) ^ (lane >> 4)) * 16), vXb = (vXa ^ 64u) + 8192 - 1024;
  const unsigned vWa = (lane >> 3) * WS_ + (c.vW2a & 127u), vWb = vWa + 8 * WS_ - 1024;
  const rsrc_t rX = make_rsrc(p.x, 2048 * 1024), rW = make_rsrc(ATT ? p.wqkv : p.w1, 512 * WS_);
  const unsigned sX = (unsigned)(rg * 128 + 16 * w) * 1024;
  const unsigned sW = (unsigned)(16 * w) * WS_ + (ATT ? 128 * cg : 448 * cg + ((cg & 1) ? 64 : 0));
  const unsigned sW3 = sW + ((cg & 1) ? -128 : 384);
  const unsigned sAct = (unsigned)WS_ACT + ((rg * 4 + tg) * 144 + 14 * cg + 2 * (grp + (cg & 1))) * 1024;
  const unsigned l16 = lane * 16;

  auto issue = [&](auto I) __attribute__((always_inline)) {
    constexpr int i = decltype(I)::value;
    if constexpr (i < N1) {
      if constexpr (it_isx(i)) dma2<slot_of(i), it_x(i) * 128u>(vXa, vXb, rX, sX, c.wdst);
      else {
        constexpr int pi = it_p(i), kq = it_q(i);
        constexpr unsigned poff = ATT ? (pi == 0 ? 2048 : pi == 1 ? 1024 : 0) : (pi < 3 ? 128 * pi : 0);
        dma2<slot_of(i), poff + kq * 128 * WS_>(vWa, vWb, rW, (!ATT && pi == 3) ? sW3 : sW, c.wdst);
      }
    } else if constexpr (i < N1 + S2N) issue_s2<i - N1, slot_of(i)>(c, c.wdst);
  };
  CSTAMP(14);
  static_for<6>(issue);
  CSTAMP(15);
  v2u ep;
  asm volatile("s_load_dwordx2 %0, %1, 0x2000" : "=s"(ep) : "s"(p.ws) : "memory");
  static_assert(WS_EPOCH == 0x2000, "epoch offset");

  v4u xf[32], a[4], b[4], c0, c1;
  f32x16 acc[2];
  float hv[8], tv[4];
  f32x4 cs4 = {0.f, 0.f, 0.f, 0.f}, sx4 = {0.f, 0.f, 0.f, 0.f};
  const unsigned sel1 = ((lane & 31) == 0 || (lane & 31) == 20) ? ONES2 : 0u;
  const v4u sel = {sel1, sel1, sel1, sel1};
  v4u mufrag = {0u, 0u, 0u, 0u};
  const unsigned tok = 32 * tg + m;
  const unsigned xa = tok * 128 + ((hh ^ ((tok >> 1) & 7)) * 16);
  const unsigned trb = (8 * hh + q4) * 128 + ((lane >> 4) & 1) * 32 + (lane & 3) * 8;
  unsigned tra = trb + ((grp ^ (q4 >> 1)) * 64);
  const unsigned csr = L_CS + grp * 1024 + lane * 16;
  const unsigned csw = hh ? (unsigned)L_KVA + m * 8 : L_CS + grp * 1024 + (tg >> 1) * 512 + m * 16 + (tg & 1) * 8;
  unsigned base = 0;
  acc[1] = zero16();

  auto mean_fix = [&](f32x16& A, int par) __attribute__((always_inline)) { A = mfma32(*(LAS v4u*)(size_t)(csr + par * 2048), mufrag, A); };
  auto RC = [&]() __attribute__((always_inline)) { const unsigned cb = base + tg * 4096; c0 = trfrag(cb, cb + 512); c1 = trfrag(cb + 2048, cb + 2560); };
  auto RB = [&](int s) __attribute__((always_inline)) { b[s] = trfrag(base + s * 2048, base + s * 2048 + 512); };
  auto RA = [&](int s) __attribute__((always_inline)) { a[s] = trfrag(base + (4 + s) * 2048, base + (4 + s) * 2048 + 512); };
  auto MU = [&](int up, int uq, int s) __attribute__((always_inline)) { acc[up & 1] = mfma32(a[s], xf[8 * uq + 4 + s], acc[up & 1]); };
  auto MR = [&](int rp, int rq, int s) __attribute__((always_inline)) { acc[rp & 1] = mfma32(b[s], xf[8 * rq + s], (rq == 0 && s == 0) ? zero16() : acc[rp & 1]); };
  auto CS = [&]() __attribute__((always_inline)) { cs4 = mfma16(sel, c0, cs4); cs4 = mfma16(sel, c1, cs4); };
  auto CSW = [&](int rp) __attribute__((always_inline)) {
    const float cs = cs4[0];
    const unsigned hi = pk2(cs, cs);
    const float lo = cs - __uint_as_float(hi << 16);
    *(LAS v2u*)(size_t)(csw + (rp & 1) * 2048) = v2u{hi, pk2(lo, lo)};
    cs4[0] = 0.f;
  };

  static_for<4>([&](auto Q) __attribute__((always_inline)) {
    constexpr int q = decltype(Q)::value;
    SB();
    CSTAMP(16 + 2 * q);
    sync<6>();
    CSTAMP(16 + 2 * q + 1);
    if constexpr (q == 0) {
      asm volatile("" : "+s"(ep));
      c.tag = v2u{ep.x + 1u, ep.y};
      if (threadIdx.x < 24) *(LAS v4u*)(size_t)(L_MBH + threadIdx.x * 16) = v4u{ep.x, 0u, ep.x, 0u};
    }
    auto D = [&](auto K) __attribute__((always_inline)) { SB(); issue(IC<3 * q + 6 + decltype(K)::value>{}); SB(); };
    unsigned xb = xa + slot_of(3 * q); TIE(xb);
    base = slot_of(3 * q + 2) + tra; TIE(base);
#pragma unroll
    for (int s = 0; s < 4; ++s) xf[8 * q + s] = *(LAS v4u*)(size_t)(xb ^ (32 * s));
    RB(0); RB(1);
    if constexpr (q >= 1) MU(0, q - 1, 0);
    D(IC<0>{});
    RB(2); RB(3);
    RC();
    if constexpr (q >= 1) MU(0, q - 1, 1);
    D(IC<1>{});
#pragma unroll
    for (int s = 0; s < 4; ++s) xf[8 * q + 4 + s] = *(LAS v4u*)(size_t)((xb ^ (32 * s)) + SLOT);
    if constexpr (q >= 1) MU(0, q - 1, 2);
    D(IC<2>{});
    if constexpr (q == 0 && ATT) { if (w == 7) dma2<L_PHI, 0u>(l16, l16, make_rsrc(p.wphi, 16384), cg * 2048, 0u); }
    if constexpr (q >= 1) MU(0, q - 1, 3);
    RA(0); RA(1); SB();
    RA(2); RA(3); MR(0, q, 0);
    CS();
    SB();
    MR(0, q, 1);
    MR(0, q, 2); MR(0, q, 3); SB();
    if (grp == 0) {
#pragma unroll
      for (int s = 0; s < 8; ++s) sx4 = mfma16(sel, xf[8 * q + s], sx4);
      TIE(sx4);
      if constexpr (q == 3) *(LAS float*)(size_t)((hh == 0 ? L_ST : L_KVA) + tok * 4) = sx4[0];
    } else {
#pragma unroll
      for (int s = 0; s < 8; ++s) acc[1] = mfma32(xf[8 * q + s], xf[8 * q + s], acc[1]);
      TIE(acc[1]);
      if constexpr (q == 3) {
        asm volatile("s_nop 15" : "+v"(acc[1]));
        const f32x16 g = acc[1];
        const float d0 = lsel<0xCC>(lsel<0xAA>(g[0], g[1]), lsel<0xAA>(g[2], g[3])), d1 = lsel<0xCC>(lsel<0xAA>(g[4], g[5]), lsel<0xAA>(g[6], g[7]));
        const float d2 = lsel<0xCC>(lsel<0xAA>(g[8], g[9]), lsel<0xAA>(g[10], g[11])), d3 = lsel<0xCC>(lsel<0xAA>(g[12], g[13]), lsel<0xAA>(g[14], g[15]));
        const float dg = lselm(lselm(d0, d1, 0xFF00FF00FF00FF00ull), lselm(d2, d3, 0xFF00FF00FF00FF00ull), 0xFFFF0000FFFF0000ull);
        *(LAS float*)(size_t)((hh == ((m >> 2) & 1) ? L_ST + 512 : L_KVA) + tok * 4) = dg;
      }
    }
    if constexpr (q == 3) CSW(0);
    if constexpr (q == 0) STAMP(1);
  });

  static_for<N1 + 1 - 12>([&](auto I) __attribute__((always_inline)) {
    constexpr int i = decltype(I)::value + 12;
    constexpr int nstd = 2 * (issued_before(ATT, i) - i);
    constexpr int nw = (ATT && i == 19) ? 6 : (!ATT && (i == 20 || i == 24)) ? 6 : nstd;
    SB();
    CSTAMP(2 * i);
    sync<(nw < nstd ? nw : nstd)>();
    CSTAMP(2 * i + 1);
    constexpr int up = it_p(i - 1), uq = it_q(i - 1), rp = it_p(i), rq = it_q(i);
    constexpr int ei = i - 13;
    constexpr int eb = ei >= 0 ? ei / 4 : -1, eu = ei >= 0 ? ei % 4 : 0;
    constexpr bool epi = eb >= 0 && (ATT ? eb == 0 : eb < 3);
    constexpr int si = i - 14, sb = si >= 0 ? si / 4 : -1, su = si >= 0 ? si % 4 : 0;
    constexpr bool sto = !ATT && sb >= 0 && sb < 3 && (su & 1);
    constexpr int i0 = i == 12 ? 18 : (ATT && i == 18) ? 22 : (ATT && i == 19) ? 25 : i + 8;
    constexpr int ni = (i == 12 || (ATT && (i == 18 || i == 19))) ? 3 : (ATT && i >= 14 && i <= 17) ? 0 : 1;
    constexpr bool own = true, ownu = true;
    if constexpr (ATT && i == 19) flag_store_m(0u, p.ws + WS_KF + cg * 128 + rg * 8, c.tag, c.m1);
    if constexpr (!ATT && i == 20) flag_store_m(0u, p.ws + WS_HF + rg * 256 + cg * 8, c.tag, c.m1);
    if constexpr (!ATT && i == 24) flag_store_m(0u, p.ws + WS_HF + rg * 256 + 64 + cg * 8, c.tag, c.m1);
    if constexpr (i == N1 - 1 || i == N1) {
      dma1_sc1_m(l16, p.ws + WS_HF + (c.rt >> 1) * 256, L_MBH, c.m16);
      if constexpr (ATT && i == N1) dma1_sc1_m(l16, p.ws + WS_KF + cg * 128, L_MBK, c.m16 >> 8);
    }
    if constexpr (!ATT && i == 20) tra = trb + (((cg & 1) ^ (q4 >> 1)) * 64);

    auto body = [&](auto UU, auto RR) __attribute__((always_inline)) {
      constexpr bool U = decltype(UU)::value, R = decltype(RR)::value;
      auto D = [&](auto K) __attribute__((always_inline)) {
        constexpr int k = decltype(K)::value;
        if constexpr (ni == 1 ? k == 2 : k < ni) { SB(); issue(IC<i0 + (ni == 1 ? 0 : k)>{}); }
        SB();
      };
      auto VA = [&](int k) __attribute__((always_inline)) {
        if constexpr (epi) {
          if constexpr (ATT) hv[4 * (eu & 1) + k] = fmul(acc[eb & 1][4 * eu + k], c.lna);
          else { tv[k] = silu_a(acc[eb & 1][4 * eu + k], c); if constexpr (eu == 3) { hv[4 + k] = acc[eb & 1][12 + k]; TIE(hv[4 + k]); } }
        }
      };
      auto VB = [&](int k) __attribute__((always_inline)) { if constexpr (epi && !ATT) tv[k] = __builtin_amdgcn_rcpf(fmaf(tv[k], c.lni, c.lni)); };
      auto VC = [&](int k) __attribute__((always_inline)) { if constexpr (epi && !ATT) hv[4 * (eu & 1) + k] = fmul(eu == 3 ? hv[4 + k] : acc[eb & 1][4 * eu + k], tv[k]); };
      if constexpr (R) { base = slot_of(i) + tra; TIE(base); if constexpr (own) RC(); RB(0); }
      if constexpr (U) MU(up, uq, 0);
      if constexpr (sto) bstore16_sc1<(4 * sb + (su >> 1)) * 1024u>(v4u{pk2(hv[0], hv[1]), pk2(hv[2], hv[3]), pk2(hv[4], hv[5]), pk2(hv[6], hv[7])}, l16, c.rWs, sAct);
      D(IC<0>{});
      if constexpr (R) { RB(1); RB(2); }
      if constexpr (U) MU(up, uq, 1);
      if constexpr (i == 12) {
        const float mean = *(LAS float*)(size_t)(L_ST + tok * 4) * (1.f / 512.f);
        const float var = fmaxf(*(LAS float*)(size_t)(L_ST + 512 + tok * 4) * (1.f / 512.f) - mean * mean, 0.f) + 1e-5f;
        c.lna = __builtin_amdgcn_rsqf(var); c.lni = var * c.lna; c.lnz = -LOG2E * c.lna;
        const float mh = __uint_as_float(pk2(-mean, 0.f) << 16);
        const unsigned mu = pk2(mh, -mean - mh);
        mufrag = v4u{mu, mu, mu, mu};
      }
      VA(0); VA(1); D(IC<1>{});
      if constexpr (R) { RB(3); RA(0); }
      if constexpr (U) MU(up, uq, 2);
      VA(2); VA(3); SB();
      if constexpr (R) RA(1);
      if constexpr (U) MU(up, uq, 3);
      if constexpr (R && own) CS();
      VB(0); VB(1); SB();
      if constexpr (U && ownu && uq == 3) mean_fix(acc[up & 1], up & 1);
      if constexpr (R) { RA(2); RA(3); MR(rp, rq, 0); }
      VC(0); VC(1); SB();
      if constexpr (R) MR(rp, rq, 1);
      VB(2); VB(3); D(IC<2>{});
      if constexpr (R) { MR(rp, rq, 2); if constexpr (own && rq == 3) CSW(rp); }
      VC(2); VC(3);
      if constexpr (ATT && epi && (eu & 1)) ph[0][eu >> 1] = v4u{pk2(hv[0], hv[1]), pk2(hv[2], hv[3]), pk2(hv[4], hv[5]), pk2(hv[6], hv[7])};
      SB();
      if constexpr (R) MR(rp, rq, 3);
      SB();
    };
    using T = std::true_type; using F = std::false_type;
    if constexpr (!ATT && i >= 20) {
      if (grp == 0) body(T{}, std::integral_constant<bool, (i < N1)>{});
      else body(std::integral_constant<bool, (up < 3)>{}, F{});
    } else body(T{}, std::integral_constant<bool, (i < N1)>{});
    if constexpr (!ATT && i == N1)
      bstore16_sc1<(4 * 2 + 1) * 1024u>(v4u{pk2(hv[0], hv[1]), pk2(hv[2], hv[3]), pk2(hv[4], hv[5]), pk2(hv[6], hv[7])}, l16, c.rWs, sAct);

    if constexpr (i == N1) {
      unsigned b0 = slot_of(i) + tra2; TIE(b0);
      const unsigned b1 = b0 ^ 64u;
      a2[0] = trfrag(b0, b0 + 1024); a2[1] = trfrag(b1, b1 + 1024); a2[2] = trfrag(b0 + 2048, b0 + 3072); a2[3] = trfrag(b1 + 2048, b1 + 3072);
    }
    if constexpr (ATT && i == 16) {
      {
        const unsigned f = (tok >> 2) & 3;
#pragma unroll
        for (int hd = 0; hd < 2; ++hd) {
          const unsigned va = slot_of(15) + (2 * grp + hd) * 4096 + tok * 32;
          *(LAS v2u*)(size_t)(va + ((hh ^ f) * 8)) = v2u{ph[0][hd].x, ph[0][hd].y};
          *(LAS v2u*)(size_t)(va + (((2 + hh) ^ f) * 8)) = v2u{ph[0][hd].z, ph[0][hd].w};
        }
      }
      phi_block(acc[1], c, ph, [](auto) {});
      const unsigned f = (tok >> 1) & 7;
#pragma unroll
      for (int hd = 0; hd < 2; ++hd) {
        const unsigned pa = slot_of(13) + grp * SLOT + hd * 8192 + tok * 64;
        *(LAS v2u*)(size_t)(pa + (((0 + hh) ^ f) * 8)) = v2u{ph[hd][0].x, ph[hd][0].y};
        *(LAS v2u*)(size_t)(pa + (((2 + hh) ^ f) * 8)) = v2u{ph[hd][0].z, ph[hd][0].w};
        *(LAS v2u*)(size_t)(pa + (((4 + hh) ^ f) * 8)) = v2u{ph[hd][1].x, ph[hd][1].y};
        *(LAS v2u*)(size_t)(pa + (((6 + hh) ^ f) * 8)) = v2u{ph[hd][1].z, ph[hd][1].w};
      }
    }
    if constexpr (ATT && i == 17) {
      if (grp == 0) {
        f32x16 kv = zero16();
        f32x4 z4 = {0.f, 0.f, 0.f, 0.f};
        const unsigned gi1 = (lane >> 4) & 1, p4 = lane & 3;
        unsigned pa0 = slot_of(13) + tg * 8192 + (8 * hh + q4) * 64 + (((4 * gi1 + p4) ^ (4 * hh + (q4 >> 1))) * 8);
        unsigned va0 = slot_of(15) + tg * 4096 + (8 * hh + q4) * 32 + ((p4 ^ (2 * hh)) * 8);
        TIE(pa0); TIE(va0);
        const unsigned pa1 = (pa0 ^ 16u) + 256;
        const unsigned va1 = (va0 ^ 8u) + 128;
#pragma unroll
        for (int h2 = 0; h2 < 2; ++h2) {
          v4u pa[4], vb[4];
#pragma unroll
          for (int s = 0; s < 4; ++s) { pa[s] = trfrag(pa0 + (4 * h2 + s) * 1024, pa1 + (4 * h2 + s) * 1024); vb[s] = trfrag(va0 + (4 * h2 + s) * 512, va1 + (4 * h2 + s) * 512); }
          SB();
#pragma unroll
          for (int s = 0; s < 4; ++s) { kv = mfma32(pa[s], vb[s], kv); z4 = mfma16(sel, pa[s], z4); }
          SB();
        }
        const unsigned sKV = (unsigned)WS_KVP + ((4 * cg + tg) * 16 + rg) * 2048, sZ = (unsigned)WS_ZP + ((4 * cg + tg) * 16 + rg) * 128;
        const unsigned vo = (hh * 16 + m) * 16, vz = lane * 4;
        asm volatile("s_nop 15\n\ts_mov_b64 exec, %7\n\t"
                     "buffer_store_dwordx4 %0, %5, %6, %8 offen sc1\n\tbuffer_store_dwordx4 %1, %5, %6, %8 offen offset:512 sc1\n\t"
                     "buffer_store_dwordx4 %2, %5, %6, %8 offen offset:1024 sc1\n\tbuffer_store_dwordx4 %3, %5, %6, %8 offen offset:1536 sc1\n\t"
                     "s_mov_b64 exec, %10\n\tbuffer_store_dword %4, %9, %6, %11 offen sc1\n\ts_mov_b64 exec, -1\n\ts_nop 0"
                     ::"v"(__builtin_shufflevector(kv, kv, 0, 1, 2, 3)), "v"(__builtin_shufflevector(kv, kv, 4, 5, 6, 7)), "v"(__builtin_shufflevector(kv, kv, 8, 9, 10, 11)),
                       "v"(__builtin_shufflevector(kv, kv, 12, 13, 14, 15)), "v"(z4[0]), "v"(vo), "s"(c.rWs), "s"(0x0000FFFF0000FFFFull), "s"(sKV), "v"(vz), "s"(0x00000000FFFFFFFFull), "s"(sZ)
                     : "memory");
      }
    }
    if constexpr (i == 12 || i == 16 || i == 20 || i == 24) STAMP(i / 4 - 1);
  });
  last = acc[ATT ? 0 : 1];
}

__global__ void __launch_bounds__(512, 2) fused(Params p) {
  extern __shared__ __attribute__((aligned(16))) char smem[];
  if ((unsigned)(size_t)smem != 0u) return;
  asm volatile("" ::"s"(p.x), "s"(p.wqkv), "s"(p.wphi), "s"(p.wo), "s"(p.w1), "s"(p.w2), "s"(p.out), "s"(p.ws));
  Ctx c;
  const int tid = threadIdx.x;
  c.lane = tid & 63;
  c.w = __builtin_amdgcn_readfirstlane(tid >> 6);
  c.tg = c.w & 3; c.grp = c.w >> 2;
  const int grp = c.grp;
  const int bid = blockIdx.x, xcd = bid & 7, idx = bid >> 3;
  const int att = idx & 1;
  c.rg = 8 * (xcd & 1) + ((idx >> 1) & 7);
  c.cg = 2 * (xcd >> 1) + (idx >> 4);
  c.rt = 4 * xcd + (idx >> 3); c.ct = idx & 7;
  c.wdst = (unsigned)c.w * 2048;
  c.m16 = (unsigned)__builtin_amdgcn_readfirstlane(tid < 64 ? 0xffff : 0); c.m1 = c.m16 >> 15;
  c.vW2a = (c.lane >> 3) * 1024 + ((c.lane & 7) ^ (((c.lane >> 4) & 1) << 2)) * 16;
  c.vW2b = c.vW2a + 8192 - 1024;
  c.s2off = (unsigned)(448 * (c.w >> 1) + 16 * (c.w & 1)) * 1024 + 128 * c.ct;
  c.s2offo = (unsigned)(128 * (c.w >> 1) + 16 * (c.w & 1)) * 1024 + 128 * c.ct;
  c.rW2 = make_rsrc(p.w2, 1792 * 1024); c.rWo = make_rsrc(p.wo, 512 * 1024); c.rWs = make_rsrc(p.ws, (unsigned)WS_END);
  STAMP(0);
  const int kq = c.tg, th = grp;
  const unsigned tra2 = kq * 4096 + (4 * (c.lane >> 5) + ((c.lane & 15) >> 2)) * 128 + (((c.lane & 15) >> 3) * 64) + ((c.lane >> 4) & 1) * 32 + (c.lane & 3) * 8;
  v4u ph[2][2], a2[4];
  f32x16 last;
  if (att) stage1<true>(p, c, ph, a2, last, tra2); else stage1<false>(p, c, ph, a2, last, tra2);
  STAMP(6);
  CSTAMP(62); CSTAMP(63);

  int ln = c.lane; TIE(ln); c.lane = ln;
  const int w = c.w;
  const unsigned m = ln & 31, hh = ln >> 5;
  const char* hfsrc = p.ws + WS_HF + (c.rt >> 1) * 256;
  const unsigned sH = (unsigned)WS_ACT + ((2 * c.rt + th) * 144 + 28 * kq) * 1024;
  const unsigned sO = (unsigned)WS_ACT + ((2 * c.rt + th) * 144 + 112 + 8 * kq) * 1024;
  const unsigned sMine = (unsigned)WS_ACT + (c.rg * 4 + c.tg) * 144 * 1024;
  const unsigned l16 = (unsigned)ln * 16;
  const unsigned t8 = (unsigned)(w * 8 + (ln >> 3)), c8 = ln & 7;
  unsigned cur = att ? slot_of(20) : slot_of(24);
  if (att) {
    const char* kfsrc = p.ws + WS_KF + c.cg * 128;
    asm volatile("" : "+s"(kfsrc));
    wait_mailbox(L_MBK, 8, kfsrc, c.tag, ln, w);
    STAMP(7);
    v4u part[16], zp;
    const unsigned sK = (unsigned)WS_KVP + ((4 * c.cg + (w >> 1)) * 16) * 2048 + (w & 1) * 1024;
    zp = gload16_sc1((ln >> 2) * 128 + (ln & 3) * 16, p.ws + WS_ZP + (size_t)((4 * c.cg + (w >> 1)) * 16) * 128 + (w & 1) * 64);
    static_for<2>([&](auto R) __attribute__((always_inline)) { constexpr int r = decltype(R)::value; bload16x2s_sc1<r * 4096u>(part[2 * r], part[2 * r + 1], l16, c.rWs, sK); });
    phi_block(last, c, ph, [&](auto K) __attribute__((always_inline)) {
      SB();
      static_for<3>([&](auto R) __attribute__((always_inline)) { constexpr int r = 2 + 3 * decltype(K)::value + decltype(R)::value; bload16x2s_sc1<r * 4096u>(part[2 * r], part[2 * r + 1], l16, c.rWs, sK); });
      SB();
    });
    asm volatile("s_waitcnt vmcnt(0)" : "+v"(part[0]), "+v"(part[1]), "+v"(part[2]), "+v"(part[3]), "+v"(part[4]), "+v"(part[5]), "+v"(part[6]), "+v"(part[7]), "+v"(zp),
                   "+v"(ph[0][0]), "+v"(ph[0][1]), "+v"(ph[1][0]), "+v"(ph[1][1])::"memory");
    asm volatile("" : "+v"(part[8]), "+v"(part[9]), "+v"(part[10]), "+v"(part[11]), "+v"(part[12]), "+v"(part[13]), "+v"(part[14]), "+v"(part[15]));
    f32x4 s4 = __builtin_bit_cast(f32x4, part[0]);
#pragma unroll
    for (int r = 1; r < 16; ++r) s4 += __builtin_bit_cast(f32x4, part[r]);
    const int ch = (w & 1) * 64 + ln, i = ch >> 5, h2 = (ch >> 4) & 1, cc = ch & 15;
    *(LAS v2u*)(size_t)(L_KVA + ((((w >> 1) * 2 + (i >> 1)) * 2 + h2) * 17 + cc) * 16 + (i & 1) * 8) = v2u{pk2(s4[0], s4[1]), pk2(s4[2], s4[3])};
    f32x4 zs = __builtin_bit_cast(f32x4, zp);
#pragma unroll
    for (int k = 0; k < 4; ++k) {
      float v = zs[k];
      v += __uint_as_float(__builtin_amdgcn_update_dpp(0u, __float_as_uint(v), 0x124, 0xf, 0xf, false));
      v += __uint_as_float(__builtin_amdgcn_update_dpp(0u, __float_as_uint(v), 0x128, 0xf, 0xf, false));
      auto r = __builtin_amdgcn_permlane16_swap(__float_as_uint(v), __float_as_uint(v), false, false);
      zs[k] = swap32_add(__uint_as_float(r[0]) + __uint_as_float(r[1]));
    }
    const int zc = (w & 1) * 4 + (ln & 3), zi = zc >> 1, z2 = zc & 1;
    *(LAS v2u*)(size_t)(L_KVA + ((((w >> 1) * 2 + (zi >> 1)) * 2 + z2) * 17 + 16) * 16 + (zi & 1) * 8) = v2u{pk2(zs[0], zs[1]), pk2(zs[2], zs[3])};
    bar();
    const unsigned sL = sMine + (112 + 4 * c.cg + 2 * grp) * 1024;
    static_for<2>([&](auto HD) __attribute__((always_inline)) {
      constexpr int hd = decltype(HD)::value;
      const unsigned ka = L_KVA + (((2 * grp + hd) * 4 + hh) * 17 + (m < 16 ? m : 16)) * 16;
      const v4u k0 = *(LAS v4u*)(size_t)(ka), k1 = *(LAS v4u*)(size_t)(ka + 2 * 17 * 16);
      f32x16 o = mfma32(k0, ph[hd][0], zero16());
      o = mfma32(k1, ph[hd][1], o);
      const float r = __builtin_amdgcn_rcpf(o[8] + 1e-6f);
      bstore16_sc1<hd * 1024u>(v4u{pk2(o[0] * r, o[1] * r), pk2(o[2] * r, o[3] * r), pk2(o[4] * r, o[5] * r), pk2(o[6] * r, o[7] * r)}, l16, c.rWs, sL);
    });
  } else if (grp == 0) {
    const unsigned sL = sMine + (14 * c.cg + ((c.cg & 1) ? 0 : 12)) * 1024;
    static_for<2>([&](auto UU) __attribute__((always_inline)) {
      constexpr int u = decltype(UU)::value;
      float hv[8];
#pragma unroll
      for (int k = 0; k < 8; ++k) hv[k] = silu_b(last[8 * u + k], silu_a(last[8 * u + k], c), c);
      bstore16_sc1<u * 1024u>(v4u{pk2(hv[0], hv[1]), pk2(hv[2], hv[3]), pk2(hv[4], hv[5]), pk2(hv[6], hv[7])}, l16, c.rWs, sL);
    });
  }
  wait_mailbox(L_MBH, 4, hfsrc, c.tag, ln, w);
  STAMP(8);
  v4u hb[S2N][2], eflag, xres;
  static_for<4>([&](auto J) __attribute__((always_inline)) { constexpr int j = decltype(J)::value; bload16x2_sc1<s2_id(j) * 2048u>(hb[j][0], hb[j][1], l16, c.rWs, sH); });
  SB();
  f32x16 acc0 = zero16(), acc1 = zero16();
  static_for<S2N>([&](auto J) __attribute__((always_inline)) {
    constexpr int j = decltype(J)::value;
    constexpr int nw = s2_wait(j);
    SB();
    CSTAMP(64 + 2 * j);
    if constexpr (j <= S2_LASTBAR) asm volatile("s_waitcnt vmcnt(%2) lgkmcnt(0)\n\ts_barrier" : "+v"(hb[j][0]), "+v"(hb[j][1]) : "i"(nw) : "memory");
    else asm volatile("s_waitcnt vmcnt(%2)" : "+v"(hb[j][0]), "+v"(hb[j][1]) : "i"(nw) : "memory");
    CSTAMP(64 + 2 * j + 1);
    if constexpr (j == 0) flag_store_m(0u, p.ws + WS_HF + c.rg * 256 + 128 + att * 64 + c.cg * 8, c.tag, c.m1);
    if constexpr (j + 9 < S2N) issue_s2<(j + 9 < S2N ? j + 9 : 0), 0u>(c, cur + c.wdst);
    if constexpr (j < S2_O) dma1_sc1_m(l16, hfsrc, L_MBH, c.m16);
    if constexpr (j == S2_B) { STAMP(9); wait_mailbox(L_MBH + 64, 4, hfsrc + 64, c.tag, ln, w); STAMP(10); }
    if constexpr (j == S2_C) { STAMP(11); wait_mailbox(L_MBH + 128, 4, hfsrc + 128, c.tag, ln, w); STAMP(12); }
    if constexpr (j == S2_O) { STAMP(13); wait_mailbox(L_MBH + 192, 4, hfsrc + 192, c.tag, ln, w); STAMP(14); }
    static_for<s2_cnt(j)>([&](auto TT) __attribute__((always_inline)) {
      constexpr int t = s2_first(j) + decltype(TT)::value, id = s2_id(t);
      if constexpr (id < 14) bload16x2_sc1<id * 2048u>(hb[t][0], hb[t][1], l16, c.rWs, sH);
      else bload16x2_sc1<(id - 14) * 2048u>(hb[t][0], hb[t][1], l16, c.rWs, sO);
    });
    if constexpr (j == 6) xres = gload16(t8 * 1024 + c8 * 16, p.x + (size_t)(64 * c.rt) * 1024 + 128 * c.ct);
    if constexpr (j == 11) { if (bid == 0 && w == 1) eflag = gload16_sc1((ln >> 2) * 256 + (ln & 3) * 16, p.ws + WS_HF); }
    cur = cur == 8 * SLOT ? 0u : cur + SLOT;
    SB();
    {
      unsigned b0 = cur + tra2; TIE(b0);
      const unsigned b1 = b0 ^ 64u;
      v4u an[4];
      acc0 = mfma32(a2[0], hb[j][0], acc0);
      if constexpr (j + 1 < S2N) { an[0] = trfrag(b0, b0 + 1024); an[1] = trfrag(b1, b1 + 1024); }
      SB();
      acc1 = mfma32(a2[1], hb[j][0], acc1);
      if constexpr (j + 1 < S2N) { an[2] = trfrag(b0 + 2048, b0 + 3072); an[3] = trfrag(b1 + 2048, b1 + 3072); }
      SB();
      acc0 = mfma32(a2[2], hb[j][1], acc0);
      acc1 = mfma32(a2[3], hb[j][1], acc1);
      if constexpr (j + 1 < S2N) { a2[0] = an[0]; a2[1] = an[1]; a2[2] = an[2]; a2[3] = an[3]; }
      asm volatile("" : "+v"(acc0), "+v"(acc1));
      SB();
    }
  });
  if (bid == 0 && w == 1) {
    asm volatile("s_waitcnt vmcnt(0)" : "+v"(eflag)::"memory");
    for (unsigned spin = 0; spin < SPIN_CAP; ++spin) {
      if (__all(eflag.x == c.tag.x && eflag.y == c.tag.y && eflag.z == c.tag.x && eflag.w == c.tag.y)) break;
      eflag = gload16_sc1((ln >> 2) * 256 + (ln & 3) * 16, p.ws + WS_HF);
      asm volatile("s_waitcnt vmcnt(0)" : "+v"(eflag)::"memory");
    }
    if (ln == 0) gstore8_sc1(0u, p.ws + WS_EPOCH, c.tag);
  }
  CSTAMP(100);
  bar();
  CSTAMP(101);
  STAMP(15);
  {
    const unsigned ra = kq * 17408 + (32 * th + m) * 272 + hh * 16;
#pragma unroll
    for (int i = 0; i < 4; ++i) {
      *(LAS f32x4*)(size_t)(ra + i * 32) = f32x4{acc0[4 * i], acc0[4 * i + 1], acc0[4 * i + 2], acc0[4 * i + 3]};
      *(LAS f32x4*)(size_t)(ra + 128 + i * 32) = f32x4{acc1[4 * i], acc1[4 * i + 1], acc1[4 * i + 2], acc1[4 * i + 3]};
    }
  }
  bar();
  {
    const unsigned ra = t8 * 272 + c8 * 32;
    f32x4 lo = (*(LAS f32x4*)(size_t)(ra) + *(LAS f32x4*)(size_t)(ra + 17408)) + (*(LAS f32x4*)(size_t)(ra + 2 * 17408) + *(LAS f32x4*)(size_t)(ra + 3 * 17408));
    f32x4 hi = (*(LAS f32x4*)(size_t)(ra + 16) + *(LAS f32x4*)(size_t)(ra + 17408 + 16)) + (*(LAS f32x4*)(size_t)(ra + 2 * 17408 + 16) + *(LAS f32x4*)(size_t)(ra + 3 * 17408 + 16));
    TIE(xres);
    const v4u o = v4u{pk2(lo[0] + __uint_as_float(xres.x << 16), lo[1] + __uint_as_float(xres.x & 0xffff0000u)),
                      pk2(lo[2] + __uint_as_float(xres.y << 16), lo[3] + __uint_as_float(xres.y & 0xffff0000u)),
                      pk2(hi[0] + __uint_as_float(xres.z << 16), hi[1] + __uint_as_float(xres.z & 0xffff0000u)),
                      pk2(hi[2] + __uint_as_float(xres.w << 16), hi[3] + __uint_as_float(xres.w & 0xffff0000u))};
    gstore16(t8 * 1024 + c8 * 16, p.out + (size_t)(64 * c.rt) * 1024 + 128 * c.ct, o);
  }
  STAMP(16);
  CSTAMP(102);
}
}

extern "C" void kernel_launch(void* const* d_in, const int* in_sizes, int n_in, void* d_out, int out_size, void* d_ws, size_t ws_size,
                              hipStream_t stream) {
  hh::Params p;
  p.x = (const char*)d_in[0]; p.wqkv = (const char*)d_in[1]; p.wphi = (const char*)d_in[2]; p.wo = (const char*)d_in[3];
  p.w1 = (const char*)d_in[5]; p.w2 = (const char*)d_in[6]; p.out = (char*)d_out; p.ws = (char*)d_ws;
  (void)hipFuncSetAttribute((const void*)hh::fused, hipFuncAttributeMaxDynamicSharedMemorySize, hh::LDS_BYTES);
  hh::fused<<<256, 512, hh::LDS_BYTES, stream>>>(p);
}
```

```cpp
#include <hip/hip_runtime.h>
#include <hip/hip_bf16.h>

#define ROT1 ((rg + (ATT ? 2 : 0) + (cg & 1)) & 3)
#define ROT2 ((c.rt + c.ct) & 3)
namespace hh {
#define LAS __attribute__((address_space(3)))
#define GAS __attribute__((address_space(1)))
#define DI __device__ __forceinline__
typedef unsigned v4u __attribute__((ext_vector_type(4)));
typedef unsigned v2u __attribute__((ext_vector_type(2)));
typedef float f32x2 __attribute__((ext_vector_type(2)));
typedef float f32x4 __attribute__((ext_vector_type(4)));
typedef float f32x16 __attribute__((ext_vector_type(16)));
typedef short s16x4 __attribute__((ext_vector_type(4)));
typedef __bf16 bf16x2 __attribute__((ext_vector_type(2)));
typedef __bf16 bf16x8 __attribute__((ext_vector_type(8)));

constexpr int SLOT = 16384, NSLOT = 9;
constexpr int MISC = SLOT * NSLOT;
constexpr int L_ST = MISC;
constexpr int L_CS = L_ST + 2048;
constexpr int L_KVA = L_CS + 4096;
constexpr int L_PHI = L_KVA + 4352;
constexpr int L_MBH = L_PHI + 2048;
constexpr int L_MBK = L_MBH + 256;
constexpr int LDS_BYTES = 163840;
static_assert(L_MBK + 128 <= LDS_BYTES, "lds");

constexpr size_t WS_HF = 0, WS_KF = 4096, WS_EPOCH = 8192;
constexpr size_t WS_ST = 16384;
constexpr size_t WS_KVP = WS_ST + 2048 * 16;
constexpr size_t WS_ZP = WS_KVP + 32 * 16 * 2048;
constexpr size_t WS_ACT = WS_ZP + 32 * 16 * 128;
constexpr size_t WS_END = WS_ACT + (size_t)64 * 144 * 1024;
constexpr size_t WS_STAMP = 64u << 20;
#define STAMP(k)
#define CSTAMP(k)
struct Params {
  const char* x; const char* wqkv; const char* wphi; const char* wo; const char* w1; const char* w2;
  char* out; char* ws;
};

DI f32x16 mfma32(v4u a, v4u b, f32x16 c) {
  return __builtin_amdgcn_mfma_f32_32x32x16_bf16(__builtin_bit_cast(bf16x8, a), __builtin_bit_cast(bf16x8, b), c, 0, 0, 0);
}
DI f32x4 mfma16(v4u a, v4u b, f32x4 c) {
  return __builtin_amdgcn_mfma_f32_16x16x32_bf16(__builtin_bit_cast(bf16x8, a), __builtin_bit_cast(bf16x8, b), c, 0, 0, 0);
}
DI f32x16 zero16() { f32x16 z;
#pragma unroll
  for (int i = 0; i < 16; ++i) z[i] = 0.f;
  return z; }
DI unsigned pk2(float lo, float hi) { f32x2 v = {lo, hi}; return __builtin_bit_cast(unsigned, __builtin_convertvector(v, bf16x2)); }
DI unsigned pk2v(f32x2 v) { return __builtin_bit_cast(unsigned, __builtin_convertvector(v, bf16x2)); }
DI v2u trr(unsigned addr) { return __builtin_bit_cast(v2u, __builtin_amdgcn_ds_read_tr16_b64_v4i16((LAS s16x4*)(size_t)addr)); }
DI v4u trfrag(unsigned a0, unsigned a1) { v2u lo = trr(a0), hi = trr(a1); return v4u{lo.x, lo.y, hi.x, hi.y}; }
DI float dot2(unsigned a, unsigned b, float c) {
  return __builtin_amdgcn_fdot2_f32_bf16(__builtin_bit_cast(bf16x2, a), __builtin_bit_cast(bf16x2, b), c, false);
}
extern "C" __device__ float hh_fmul_legacy(float, float) __asm("llvm.amdgcn.fmul.legacy");
DI float fmul(float a, float b) { return hh_fmul_legacy(a, b); }
template <int CTRL> DI float dpp_add(float v) { return v + __uint_as_float(__builtin_amdgcn_update_dpp(0u, __float_as_uint(v), CTRL, 0xf, 0xf, false)); }
DI float row_sum16(float v) { return dpp_add<0x128>(dpp_add<0x124>(dpp_add<0x122>(dpp_add<0x121>(v)))); }
DI float lselm(float a, float b, unsigned long long mask) { float r; asm("v_cndmask_b32 %0, %1, %2, %3" : "=v"(r) : "v"(a), "v"(b), "s"(mask)); return r; }
template <unsigned B> DI float lsel(float a, float b) { return lselm(a, b, 0x0101010101010101ull * B); }
DI float swap32_add(float v) {
  auto r = __builtin_amdgcn_permlane32_swap(__float_as_uint(v), __float_as_uint(v), false, false);
  return __uint_as_float(r[0]) + __uint_as_float(r[1]);
}
DI float rows_sum(float v) {
  auto r = __builtin_amdgcn_permlane16_swap(__float_as_uint(v), __float_as_uint(v), false, false);
  return swap32_add(__uint_as_float(r[0]) + __uint_as_float(r[1]));
}
DI float swap32_max(float v) {
  auto r = __builtin_amdgcn_permlane32_swap(__float_as_uint(v), __float_as_uint(v), false, false);
  return fmaxf(__uint_as_float(r[0]), __uint_as_float(r[1]));
}
typedef __amdgpu_buffer_rsrc_t rsrc_t;
DI rsrc_t make_rsrc(const char* base, unsigned bytes) { return __builtin_amdgcn_make_buffer_rsrc((void*)base, 0, bytes, 0x00020000); }
template <unsigned LO, unsigned GO> DI void dma2(unsigned v0, unsigned v1, rsrc_t rs, unsigned sc, unsigned ld) {
  unsigned t;
  asm volatile("s_add_u32 m0, %5, %6\n\ts_add_u32 %0, %4, %7\n\tbuffer_load_dwordx4 %1, %3, %0 offen lds\n\tbuffer_load_dwordx4 %2, %3, %0 offen offset:1024 lds"
               : "=&s"(t) : "v"(v0), "v"(v1), "s"(rs), "s"(sc), "s"(ld), "i"(LO), "i"(GO) : "memory", "scc");
}
DI void dma1_sc1(unsigned v0, const char* s0, unsigned dst) {
  asm volatile("s_mov_b32 m0, %2\n\ts_nop 3\n\tglobal_load_lds_dwordx4 %0, %1 sc1" ::"v"(v0), "s"(s0), "s"(dst) : "memory");
}
DI void dma1_sc1_m(unsigned v0, const char* s0, unsigned dst, unsigned long long mask) {
  asm volatile("s_mov_b32 m0, %2\n\ts_mov_b64 exec, %3\n\tglobal_load_lds_dwordx4 %0, %1 sc1\n\ts_mov_b64 exec, -1" ::"v"(v0), "s"(s0), "s"(dst), "s"(mask) : "memory");
}
DI void flag_store_m(unsigned zero, char* sbase, v2u v, unsigned long long mask) {
  asm volatile("s_mov_b64 exec, %3\n\tglobal_store_dwordx2 %0, %1, %2 sc1\n\ts_mov_b64 exec, -1\n\ts_nop 0" ::"v"(zero), "v"(v), "s"(sbase), "s"(mask) : "memory");
}
template <int N> DI void sync() { asm volatile("s_waitcnt vmcnt(%0) lgkmcnt(0)\n\ts_barrier" ::"i"(N) : "memory"); }
DI void bar() { asm volatile("s_waitcnt lgkmcnt(0)\n\ts_barrier" ::: "memory"); }
DI v4u gload16_sc1(unsigned voff, const char* sbase) {
  v4u r; asm volatile("s_nop 4\n\tglobal_load_dwordx4 %0, %1, %2 sc1" : "=v"(r) : "v"(voff), "s"(sbase) : "memory"); return r;
}
template <unsigned GO> DI void bload16x2_sc1(v4u& a, v4u& b, unsigned voff, rsrc_t rs, unsigned sc) {
  unsigned t;
  asm volatile("s_add_u32 %2, %5, %6\n\tbuffer_load_dwordx4 %0, %3, %4, %2 offen sc1\n\tbuffer_load_dwordx4 %1, %3, %4, %2 offen offset:1024 sc1"
               : "=&v"(a), "=&v"(b), "=&s"(t) : "v"(voff), "s"(rs), "s"(sc), "i"(GO) : "memory", "scc");
}
template <unsigned GO> DI void bload16x2s_sc1(v4u& a, v4u& b, unsigned voff, rsrc_t rs, unsigned sc) {
  unsigned t;
  asm volatile("s_add_u32 %2, %5, %6\n\tbuffer_load_dwordx4 %0, %3, %4, %2 offen sc1\n\tbuffer_load_dwordx4 %1, %3, %4, %2 offen offset:2048 sc1"
               : "=&v"(a), "=&v"(b), "=&s"(t) : "v"(voff), "s"(rs), "s"(sc), "i"(GO) : "memory", "scc");
}
template <unsigned GO> DI void bstore16_sc1(v4u v, unsigned voff, rsrc_t rs, unsigned sc) {
  unsigned t;
  asm volatile("s_add_u32 %0, %4, %5\n\tbuffer_store_dwordx4 %1, %2, %3, %0 offen sc1\n\ts_nop 1" : "=&s"(t) : "v"(v), "v"(voff), "s"(rs), "s"(sc), "i"(GO) : "memory", "scc");
}
DI v4u gload16(unsigned voff, const char* sbase) {
  v4u r; asm volatile("s_nop 4\n\tglobal_load_dwordx4 %0, %1, %2" : "=v"(r) : "v"(voff), "s"(sbase) : "memory"); return r;
}
DI void gstore16_sc1(unsigned voff, char* sbase, v4u v) {
  asm volatile("s_nop 4\n\tglobal_store_dwordx4 %0, %1, %2 sc1\n\ts_nop 1" ::"v"(voff), "v"(v), "s"(sbase) : "memory");
}
DI void gstore16(unsigned voff, char* sbase, v4u v) {
  asm volatile("s_nop 4\n\tglobal_store_dwordx4 %0, %1, %2\n\ts_nop 1" ::"v"(voff), "v"(v), "s"(sbase) : "memory");
}
DI void gstore8_sc1(unsigned voff, char* sbase, v2u v) {
  asm volatile("s_nop 4\n\tglobal_store_dwordx2 %0, %1, %2 sc1\n\ts_nop 1" ::"v"(voff), "v"(v), "s"(sbase) : "memory");
}
#define TIE(x) asm volatile("" : "+v"(x))
#define SB() __builtin_amdgcn_sched_barrier(0)
DI void pin(f32x16& a) { asm volatile("" : "+v"(a)); }

constexpr float LOG2E = 1.4426950408889634f;
constexpr unsigned ONES2 = 0x3f803f80u;
constexpr unsigned SPIN_CAP = 1u << 16;

template <int N, class F> DI void static_for(F&& f) {
  if constexpr (N > 0) { static_for<N - 1>(f); f(std::integral_constant<int, N - 1>{}); }
}

DI void wait_mailbox(unsigned mb, int nl, const char* src, v2u tag, int lane, unsigned long long mask) {
  const unsigned a = mb + (unsigned)((lane < nl ? lane : 0) * 16);
  for (unsigned spin = 0;; ++spin) {
    const v4u f = *(volatile LAS v4u*)(size_t)a;
    const bool ok = f.x == tag.x && f.y == tag.y && f.z == tag.x && f.w == tag.y;
    if (__all(ok)) break;
    if (spin > SPIN_CAP) break;
    dma1_sc1_m((unsigned)lane * 16, src, mb, mask);
    __builtin_amdgcn_s_sleep(2);
  }
  asm volatile("" ::: "memory");
}

DI float max3(float a, float b, float c) { float r; asm("v_max3_f32 %0, %1, %2, %3" : "=v"(r) : "v"(a), "v"(b), "v"(c)); return r; }
DI float max2(float a, float b) { float r; asm("v_max_f32 %0, %1, %2" : "=v"(r) : "v"(a), "v"(b)); return r; }
DI float max8(const f32x16& P, int o) { return max2(max3(P[o], P[o + 1], P[o + 2]), max3(P[o + 3], P[o + 4], max3(P[o + 5], P[o + 6], P[o + 7]))); }
template <bool MAX> DI void cross2(float& a, float& b) {
  auto r = __builtin_amdgcn_permlane32_swap(__float_as_uint(a), __float_as_uint(b), false, false);
  const float t = MAX ? max2(__uint_as_float(r[0]), __uint_as_float(r[1])) : __uint_as_float(r[0]) + __uint_as_float(r[1]);
  unsigned t2 = __float_as_uint(t); asm volatile("s_nop 1" : "+v"(t2));
  auto s = __builtin_amdgcn_permlane32_swap(t2, t2, false, false);
  a = __uint_as_float(s[0]); b = __uint_as_float(s[1]);
}
DI void phi_softmax(const f32x16& P, const float sc, v4u& pos, v4u& neg) {
  float mp = max8(P, 0), mn = max8(P, 8);
  asm volatile("s_nop 1" : "+v"(mp), "+v"(mn));
  cross2<true>(mp, mn);
  const f32x2 s2 = {sc, sc}, cp = {-mp * sc, -mp * sc}, cn = {-mn * sc, -mn * sc};
  f32x2 e[8];
#pragma unroll
  for (int i = 0; i < 8; ++i) {
    const f32x2 a = __builtin_elementwise_fma(f32x2{P[2 * i], P[2 * i + 1]}, s2, i < 4 ? cp : cn);
    e[i] = f32x2{__builtin_amdgcn_exp2f(a.x), __builtin_amdgcn_exp2f(a.y)};
  }
  const f32x2 p2 = (e[0] + e[1]) + (e[2] + e[3]), n2 = (e[4] + e[5]) + (e[6] + e[7]);
  float sp = p2.x + p2.y, sn = n2.x + n2.y;
  cross2<false>(sp, sn);
  const float rp = __builtin_amdgcn_rcpf(sp), rn = __builtin_amdgcn_rcpf(sn);
  const f32x2 rp2 = {rp, rp}, rn2 = {rn, rn};
  pos = v4u{pk2v(e[0] * rp2), pk2v(e[1] * rp2), pk2v(e[2] * rp2), pk2v(e[3] * rp2)};
  neg = v4u{pk2v(e[4] * rn2), pk2v(e[5] * rn2), pk2v(e[6] * rn2), pk2v(e[7] * rn2)};
}

constexpr bool it_isx(int i) { return i < 12 && (i % 3) != 2; }
constexpr int it_x(int i) { return 2 * (i / 3) + (i % 3); }
constexpr int it_p(int i) { return i < 12 ? 0 : (i - 12) / 4 + 1; }
constexpr int it_q(int i) { return i < 12 ? i / 3 : (i - 12) % 4; }
constexpr int n1_of(bool att) { return att ? 20 : 24; }
constexpr unsigned slot_of(int i) { return (unsigned)(i % NSLOT) * SLOT; }
constexpr int s2_id(int j) { return j < 12 ? 2 * (j / 4) + (j & 1) + ((j & 2) ? 8 : 0) : j < 14 ? j - 6 : j; }
constexpr int S2N = 18;
constexpr int issued_before(bool att, int i) { return i == 12 ? 17 : (att && i >= 15 && i <= 18) ? 21 : (att && i == 19) ? 24 : i + 7; }
constexpr int S2_B = 0, S2_C = 2, S2_O = 8;
constexpr int s2_ld(int j) { return j < 4 ? -1 : j < 8 ? S2_B + (j - 4) / 2 : j < 12 ? S2_C + (j - 8) / 2 : j < 14 ? S2_C + 2 + (j - 12) : S2_O + (j - 14); }
constexpr int s2_wd(int t) { return t - 5; }
constexpr int s2_cnt(int k) { int n = 0; for (int j = 0; j < S2N; ++j) n += s2_ld(j) == k; return n; }
constexpr int s2_first(int k) { for (int j = 0; j < S2N; ++j) if (s2_ld(j) == k) return j; return 0; }
constexpr int s2_wat(int k) { for (int t = 9; t < S2N; ++t) if (s2_wd(t) == k) return t; return -1; }
constexpr int s2_ops(int k) { return (s2_wat(k) >= 0 ? 2 : 0) + 2 * s2_cnt(k); }
constexpr int s2_since(int k0, int k1) { int n = 0; for (int k = k0; k < k1; ++k) n += s2_ops(k); return n; }
constexpr int s2_wait(int j) {
  int n = s2_since(s2_ld(j) + 1, j);
  for (int t = j + 1; t < S2N; ++t) n += s2_ld(t) == s2_ld(j) ? 2 : 0;
  if (j + 1 >= 9 && j + 1 < S2N) { const int nd = s2_since(s2_wd(j + 1), j) - 2; if (nd < n) n = nd; }
  return n;
}
constexpr int s2_lastbar() {
  for (int j = s2_wd(S2N - 1) + 1; j < S2N; ++j) if (s2_wait(j) <= s2_since(s2_wd(S2N - 1), j) - 2) return j;
  return S2N - 1;
}
template <int V> using IC = std::integral_constant<int, V>;

struct Ctx {
  int lane, w, tg, grp, rg, cg, rt, ct;
  unsigned wdst, vW2a, vW2b;
  unsigned s2w[4], s2o[4], s2w0;
  unsigned r2h[4], r2o[4];
  rsrc_t rW2, rWo, rWs;
  v2u tag;
  unsigned long long m16, m1;
  float lna, lni, lnz;
};

template <int J, unsigned LO> DI void issue_s2(const Ctx& c, unsigned ld) {
  if constexpr (J < 12) dma2<LO, (J / 4) * 2 * 32768u>(c.vW2a, c.vW2b, c.rW2, c.s2w[J & 3], ld);
  else if constexpr (J < 14) dma2<LO, s2_id(J) * 32768u>(c.vW2a, c.vW2b, c.rW2, c.s2w0, ld);
  else dma2<LO, 0u>(c.vW2a, c.vW2b, c.rWo, c.s2o[J - 14], ld);
}
DI float silu_a(float a, const Ctx& c) { return __builtin_amdgcn_exp2f(fmul(a, c.lnz)); }
DI float silu_b(float a, float t, const Ctx& c) { return fmul(a, __builtin_amdgcn_rcpf(fmaf(t, c.lni, c.lni))); }
template <class F> DI void phi_block(const f32x16& A, const Ctx& c, v4u (&ph)[2][2], F&& between) {
  const unsigned pa = L_PHI + c.grp * 1024 + (4 * (c.lane >> 5) + ((c.lane & 15) >> 2)) * 32 + (c.lane & 3) * 8;
  const unsigned sg = (c.lane & 16) ? 0x80008000u : 0u;
  f32x16 P[2];
#pragma unroll
  for (int hd = 0; hd < 2; ++hd) {
    const v4u fr = v4u{pk2(A[8 * hd], A[8 * hd + 1]), pk2(A[8 * hd + 2], A[8 * hd + 3]), pk2(A[8 * hd + 4], A[8 * hd + 5]), pk2(A[8 * hd + 6], A[8 * hd + 7])};
    const v4u aphi = trfrag(pa + hd * 512, pa + hd * 512 + 256) ^ sg;
    P[hd] = mfma32(aphi, fr, zero16());
  }
  between(IC<0>{});
  asm volatile("s_nop 15" : "+v"(P[0]), "+v"(P[1]));
  phi_softmax(P[0], -c.lnz, ph[0][0], ph[0][1]);
  between(IC<1>{});
  phi_softmax(P[1], -c.lnz, ph[1][0], ph[1][1]);
}

template <bool ATT>
DI void stage1(const Params& p, Ctx& c, v4u (&ph)[2][2], v4u (&a2)[4], f32x16& last, const unsigned tra2) {
  constexpr int N1 = n1_of(ATT);
  constexpr unsigned WS_ = ATT ? 3072 : 3584;
  const int lane = c.lane, w = c.w, tg = c.tg, grp = c.grp, rg = c.rg, cg = c.cg;
  const unsigned m = lane & 31, hh = lane >> 5, q4 = (lane & 15) >> 2;
  const unsigned vXa = (lane >> 3) * 1024 + (((lane & 7) ^ (lane >> 4)) * 16), vXb = (vXa ^ 64u) + 8192 - 1024;
  const unsigned vWa = (lane >> 3) * WS_ + (c.vW2a & 127u), vWb = vWa + 8 * WS_ - 1024;
  const rsrc_t rX = make_rsrc(p.x, 2048 * 1024), rW = make_rsrc(ATT ? p.wqkv : p.w1, 512 * WS_);
  const unsigned rot = ROT1;
  unsigned sX[4], sW[4];
#pragma unroll
  for (int q = 0; q < 4; ++q) {
    sX[q] = (unsigned)(rg * 128 + 16 * w) * 1024 + ((q + rot) & 3) * 256;
    sW[q] = (unsigned)(16 * w) * WS_ + (ATT ? 128 * cg : 448 * cg + ((cg & 1) ? 64 : 0)) + ((q + rot) & 3) * 128 * WS_;
  }
  const unsigned vW3 = (cg & 1) ? -128 : 384;
  const unsigned sAct = (unsigned)WS_ACT + ((rg * 4 + tg) * 144 + 14 * cg + 2 * (grp + (cg & 1))) * 1024;
  const unsigned l16 = lane * 16;

  auto issue = [&](auto I) __attribute__((always_inline)) {
    constexpr int i = decltype(I)::value;
    if constexpr (i < N1) {
      if constexpr (it_isx(i)) dma2<slot_of(i), (it_x(i) & 1) * 128u>(vXa, vXb, rX, sX[it_x(i) >> 1], c.wdst);
      else {
        constexpr int pi = it_p(i), kq = it_q(i);
        constexpr unsigned poff = ATT ? (pi == 0 ? 2048 : pi == 1 ? 1024 : 0) : (pi < 3 ? 128 * pi : 0);
        dma2<slot_of(i), poff>(vWa, vWb, rW, (!ATT && pi == 3) ? sW[kq] + vW3 : sW[kq], c.wdst);
      }
    } else if constexpr (i < N1 + S2N) issue_s2<i - N1, slot_of(i)>(c, c.wdst);
  };
  CSTAMP(14);
  v2u ep;
  asm volatile("s_load_dwordx2 %0, %1, 0x2000" : "=s"(ep) : "s"(p.ws) : "memory");
  static_assert(WS_EPOCH == 0x2000, "epoch offset");
  const unsigned srow = rg * 128 + ((ATT ? 8 : 0) + cg) * 8 + w;
  v4u xr;
  asm volatile("buffer_load_dwordx4 %0, %1, %2, %3 offen" : "=v"(xr) : "v"(l16), "s"(rX), "s"(srow * 1024) : "memory");
  static_for<3>(issue);
  asm volatile("s_barrier" ::: "memory");
  issue(IC<3>{}); issue(IC<4>{}); issue(IC<5>{});
  CSTAMP(15);
  {
    asm volatile("s_waitcnt vmcnt(12) lgkmcnt(0)" : "+v"(xr), "+s"(ep)::"memory");
    c.tag = v2u{ep.x + 1u, ep.y};
    if (threadIdx.x < 24) *(LAS v4u*)(size_t)(L_MBH + threadIdx.x * 16) = v4u{ep.x, 0u, ep.x, 0u};
    if (threadIdx.x < 128) *(LAS v4u*)(size_t)(L_ST + threadIdx.x * 16) = v4u{ep.x, ep.x, ep.x, ep.x};
    float s1 = dot2(xr.y, ONES2, dot2(xr.x, ONES2, 0.f)) + dot2(xr.w, ONES2, dot2(xr.z, ONES2, 0.f));
    float s2 = dot2(xr.y, xr.y, dot2(xr.x, xr.x, 0.f)) + dot2(xr.w, xr.w, dot2(xr.z, xr.z, 0.f));
    s1 = row_sum16(s1); s2 = row_sum16(s2);
    s1 = rows_sum(s1); s2 = rows_sum(s2);
    const float mean = s1 * (1.f / 512.f);
    const float var = fmaxf(s2 * (1.f / 512.f) - mean * mean, 0.f) + 1e-5f;
    const v4u g = {__float_as_uint(mean), c.tag.x, __float_as_uint(var), c.tag.x ^ c.tag.y ^ 0x5bd1e995u};
    asm volatile("s_mov_b64 exec, 1\n\tbuffer_store_dwordx4 %0, %1, %2, %3 offen sc1\n\ts_mov_b64 exec, -1\n\ts_nop 0"
                 ::"v"(g), "v"(l16), "s"(c.rWs), "s"((unsigned)WS_ST + srow * 16) : "memory");
  }

  v4u xf[32], a[4], b[4], c0, c1;
  f32x16 acc[2];
  float hv[8], tv[4];
  f32x4 cs4 = {0.f, 0.f, 0.f, 0.f};
  const unsigned sel1 = ((lane & 31) == 0 || (lane & 31) == 20) ? ONES2 : 0u;
  const v4u sel = {sel1, sel1, sel1, sel1};
  v4u mufrag = {0u, 0u, 0u, 0u};
  const unsigned tok = 32 * tg + m;
  const unsigned xa = tok * 128 + ((hh ^ ((tok >> 1) & 7)) * 16);
  const unsigned trb = (8 * hh + q4) * 128 + ((lane >> 4) & 1) * 32 + (lane & 3) * 8;
  unsigned tra = trb + ((grp ^ (q4 >> 1)) * 64);
  const unsigned csr = L_CS + grp * 1024 + lane * 16;
  const unsigned csw = hh ? (unsigned)L_KVA + m * 8 : L_CS + grp * 1024 + (tg >> 1) * 512 + m * 16 + (tg & 1) * 8;
  unsigned base = 0;

  auto mean_fix = [&](f32x16& A, int par) __attribute__((always_inline)) { A = mfma32(*(LAS v4u*)(size_t)(csr + par * 2048), mufrag, A); };
  auto RC = [&]() __attribute__((always_inline)) { const unsigned cb = base + tg * 4096; c0 = trfrag(cb, cb + 512); c1 = trfrag(cb + 2048, cb + 2560); };
  auto RB = [&](int s) __attribute__((always_inline)) { b[s] = trfrag(base + s * 2048, base + s * 2048 + 512); };
  auto RA = [&](int s) __attribute__((always_inline)) { a[s] = trfrag(base + (4 + s) * 2048, base + (4 + s) * 2048 + 512); };
  auto MU = [&](int up, int uq, int s) __attribute__((always_inline)) { acc[up & 1] = mfma32(a[s], xf[8 * uq + 4 + s], acc[up & 1]); };
  auto MR = [&](int rp, int rq, int s) __attribute__((always_inline)) { acc[rp & 1] = mfma32(b[s], xf[8 * rq + s], (rq == 0 && s == 0) ? zero16() : acc[rp & 1]); };
  auto CS = [&]() __attribute__((always_inline)) { cs4 = mfma16(sel, c0, cs4); cs4 = mfma16(sel, c1, cs4); };
  auto CSW = [&](int rp) __attribute__((always_inline)) {
    const float cs = cs4[0];
    const unsigned hi = pk2(cs, cs);
    const float lo = cs - __uint_as_float(hi << 16);
    *(LAS v2u*)(size_t)(csw + (rp & 1) * 2048) = v2u{hi, pk2(lo, lo)};
    cs4[0] = 0.f;
  };

  const char* stsrc = p.ws + WS_ST + rg * 2048;
  const unsigned long long mall = 0ull - c.m1;
  auto st_refresh = [&]() __attribute__((always_inline)) { dma1_sc1_m(l16, stsrc, L_ST, mall); dma1_sc1_m(l16, stsrc + 1024, L_ST + 1024, mall); };

  static_for<4>([&](auto Q) __attribute__((always_inline)) {
    constexpr int q = decltype(Q)::value;
    SB();
    CSTAMP(16 + 2 * q);
    sync<6>();
    CSTAMP(16 + 2 * q + 1);
    auto D = [&](auto K) __attribute__((always_inline)) { SB(); issue(IC<3 * q + 6 + decltype(K)::value>{}); SB(); };
    unsigned xb = xa + slot_of(3 * q); TIE(xb);
    base = slot_of(3 * q + 2) + tra; TIE(base);
#pragma unroll
    for (int s = 0; s < 4; ++s) xf[8 * q + s] = *(LAS v4u*)(size_t)(xb ^ (32 * s));
    RB(0); RB(1);
    if constexpr (q >= 1) MU(0, q - 1, 0);
    SB();
    RB(2); RB(3); RC();
    if constexpr (q >= 1) MU(0, q - 1, 1);
    D(IC<0>{});
#pragma unroll
    for (int s = 0; s < 4; ++s) xf[8 * q + 4 + s] = *(LAS v4u*)(size_t)((xb ^ (32 * s)) + SLOT);
    if constexpr (q >= 1) MU(0, q - 1, 2);
    RA(0); SB();
    if constexpr (q >= 1) MU(0, q - 1, 3);
    RA(1); D(IC<1>{});
    RA(2); RA(3); MR(0, q, 0); CS(); SB();
    MR(0, q, 1); D(IC<2>{});
    if constexpr (q == 0 && ATT) { if (w == 7) dma2<L_PHI, 0u>(l16, l16, make_rsrc(p.wphi, 16384), cg * 2048, 0u); }
    if constexpr (q >= 2) st_refresh();
    MR(0, q, 2);
    if constexpr (q == 3) CSW(0);
    MR(0, q, 3); pin(acc[0]); SB();
    if constexpr (q == 0) STAMP(1);
  });

  static_for<N1 + 1 - 12>([&](auto I) __attribute__((always_inline)) {
    constexpr int i = decltype(I)::value + 12;
    constexpr int nstd = 2 * (issued_before(ATT, i) - i);
    constexpr int nw = (ATT && i == 19) ? 6 : (!ATT && (i == 19 || i == 23)) ? 4 : nstd;
    SB();
    CSTAMP(2 * i);
    sync<(nw < nstd ? nw : nstd)>();
    CSTAMP(2 * i + 1);
    constexpr int up = it_p(i - 1), uq = it_q(i - 1), rp = it_p(i), rq = it_q(i);
    constexpr int ei = i - 13;
    constexpr int eb = ei >= 0 ? ei / 4 : -1, eu = ei >= 0 ? ei % 4 : 0;
    constexpr bool epi = eb >= 0 && (ATT ? eb == 0 : eb < 3);
    constexpr int si = i - 14, sb = si >= 0 ? si / 4 : -1, su = si >= 0 ? si % 4 : 0;
    constexpr bool sto = !ATT && sb >= 0 && sb < 3 && (su & 1);
    constexpr int i0 = i == 12 ? 18 : (ATT && i == 18) ? 22 : (ATT && i == 19) ? 25 : i + 8;
    constexpr int ni = (i == 12 || (ATT && (i == 18 || i == 19))) ? 3 : (ATT && i >= 14 && i <= 17) ? 0 : 1;
    constexpr bool own = true, ownu = true;
    if constexpr (ATT && i == 19) flag_store_m(0u, p.ws + WS_KF + cg * 128 + rg * 8, c.tag, c.m1);
    if constexpr (!ATT && i == 19) flag_store_m(0u, p.ws + WS_HF + rg * 256 + cg * 8, c.tag, c.m1);
    if constexpr (!ATT && i == 23) flag_store_m(0u, p.ws + WS_HF + rg * 256 + 64 + cg * 8, c.tag, c.m1);
    if constexpr (i >= N1 - 2) {
      dma1_sc1_m(l16, p.ws + WS_HF + (c.rt >> 1) * 256, L_MBH, c.m16);
      if constexpr (ATT && i == N1) dma1_sc1_m(l16, p.ws + WS_KF + cg * 128, L_MBK, c.m16 >> 8);
    }
    if constexpr (!ATT && i == 20) tra = trb + (((cg & 1) ^ (q4 >> 1)) * 64);

    auto body = [&](auto UU, auto RR) __attribute__((always_inline)) {
      constexpr bool U = decltype(UU)::value, R = decltype(RR)::value;
      auto D = [&](auto K) __attribute__((always_inline)) {
        constexpr int k = decltype(K)::value;
        if constexpr (ni == 1 ? k == 2 : k < ni) { SB(); issue(IC<i0 + (ni == 1 ? 0 : k)>{}); }
        SB();
      };
      auto VA = [&](int k) __attribute__((always_inline)) {
        if constexpr (epi) {
          if constexpr (ATT) hv[4 * (eu & 1) + k] = fmul(acc[eb & 1][4 * eu + k], c.lna);
          else { tv[k] = silu_a(acc[eb & 1][4 * eu + k], c); if constexpr (eu == 3) { hv[4 + k] = acc[eb & 1][12 + k]; TIE(hv[4 + k]); } }
        }
      };
      auto VB = [&](int k) __attribute__((always_inline)) { if constexpr (epi && !ATT) tv[k] = __builtin_amdgcn_rcpf(fmaf(tv[k], c.lni, c.lni)); };
      auto VC = [&](int k) __attribute__((always_inline)) { if constexpr (epi && !ATT) hv[4 * (eu & 1) + k] = fmul(eu == 3 ? hv[4 + k] : acc[eb & 1][4 * eu + k], tv[k]); };
      if constexpr (R) { base = slot_of(i) + tra; TIE(base); RC(); RB(0); }
      if constexpr (i == 12) {
        v4u g;
        for (unsigned spin = 0;; ++spin) {
          g = *(volatile LAS v4u*)(size_t)(L_ST + tok * 16);
          const bool ok = g.y == c.tag.x && g.w == (c.tag.x ^ c.tag.y ^ 0x5bd1e995u);
          bool all = true;
          if (w == 4) {
            const v4u g0 = *(volatile LAS v4u*)(size_t)(L_ST + l16), g1 = *(volatile LAS v4u*)(size_t)(L_ST + 1024 + l16);
            all = g0.y == c.tag.x && g0.w == (c.tag.x ^ c.tag.y ^ 0x5bd1e995u) && g1.y == c.tag.x && g1.w == (c.tag.x ^ c.tag.y ^ 0x5bd1e995u);
          }
          if (__all(ok && all) || spin > SPIN_CAP) break;
          st_refresh();
          __builtin_amdgcn_s_sleep(2);
        }
        const float mean = __uint_as_float(g.x), var = __uint_as_float(g.z);
        c.lna = __builtin_amdgcn_rsqf(var); c.lni = var * c.lna; c.lnz = -LOG2E * c.lna;
        const float mh = __uint_as_float(pk2(-mean, 0.f) << 16);
        const unsigned mu = pk2(mh, -mean - mh);
        mufrag = v4u{mu, mu, mu, mu};
      }
      if constexpr (U) MU(up, uq, 0);
      if constexpr (sto) bstore16_sc1<(4 * sb + (su >> 1)) * 1024u>(v4u{pk2(hv[0], hv[1]), pk2(hv[2], hv[3]), pk2(hv[4], hv[5]), pk2(hv[6], hv[7])}, l16, c.rWs, sAct);
      D(IC<0>{});
      if constexpr (R) { RB(1); RB(2); }
      if constexpr (U) MU(up, uq, 1);
      VA(0); VA(1); D(IC<1>{});
      if constexpr (R) { RB(3); RA(0); }
      if constexpr (U) MU(up, uq, 2);
      VA(2); VA(3); SB();
      if constexpr (R) RA(1);
      if constexpr (U) MU(up, uq, 3);
      if constexpr (R && own) CS();
      VB(0); VB(1); SB();
      if constexpr (U && uq == 3) mean_fix(acc[up & 1], up & 1);
      if constexpr (R) { RA(2); RA(3); MR(rp, rq, 0); }
      VC(0); VC(1); SB();
      if constexpr (R) MR(rp, rq, 1);
      VB(2); VB(3); D(IC<2>{});
      if constexpr (R) { MR(rp, rq, 2); if constexpr (own && rq == 3) CSW(rp); }
      VC(2); VC(3);
      if constexpr (ATT && epi && (eu & 1)) ph[0][eu >> 1] = v4u{pk2(hv[0], hv[1]), pk2(hv[2], hv[3]), pk2(hv[4], hv[5]), pk2(hv[6], hv[7])};
      SB();
      if constexpr (R) { MR(rp, rq, 3); pin(acc[rp & 1]); }
      SB();
    };
    using T = std::true_type; using F = std::false_type;
    if constexpr (!ATT && i >= 20) {
      if (grp == 0) body(T{}, std::integral_constant<bool, (i < N1)>{});
      else body(std::integral_constant<bool, (up < 3)>{}, F{});
    } else body(T{}, std::integral_constant<bool, (i < N1)>{});
    if constexpr (!ATT && i == N1)
      bstore16_sc1<(4 * 2 + 1) * 1024u>(v4u{pk2(hv[0], hv[1]), pk2(hv[2], hv[3]), pk2(hv[4], hv[5]), pk2(hv[6], hv[7])}, l16, c.rWs, sAct);

    if constexpr (i == N1) {
      unsigned b0 = slot_of(i) + tra2; TIE(b0);
      const unsigned b1 = b0 ^ 64u;
      a2[0] = trfrag(b0, b0 + 1024); a2[1] = trfrag(b1, b1 + 1024); a2[2] = trfrag(b0 + 2048, b0 + 3072); a2[3] = trfrag(b1 + 2048, b1 + 3072);
    }
    if constexpr (ATT && i == 16) {
      {
        const unsigned f = (tok >> 2) & 3;
#pragma unroll
        for (int hd = 0; hd < 2; ++hd) {
          const unsigned va = slot_of(15) + (2 * grp + hd) * 4096 + tok * 32;
          *(LAS v2u*)(size_t)(va + ((hh ^ f) * 8)) = v2u{ph[0][hd].x, ph[0][hd].y};
          *(LAS v2u*)(size_t)(va + (((2 + hh) ^ f) * 8)) = v2u{ph[0][hd].z, ph[0][hd].w};
        }
      }
      phi_block(acc[1], c, ph, [](auto) {});
      const unsigned f = (tok >> 1) & 7;
#pragma unroll
      for (int hd = 0; hd < 2; ++hd) {
        const unsigned pa = slot_of(13) + grp * SLOT + hd * 8192 + tok * 64;
        *(LAS v2u*)(size_t)(pa + (((0 + hh) ^ f) * 8)) = v2u{ph[hd][0].x, ph[hd][0].y};
        *(LAS v2u*)(size_t)(pa + (((2 + hh) ^ f) * 8)) = v2u{ph[hd][0].z, ph[hd][0].w};
        *(LAS v2u*)(size_t)(pa + (((4 + hh) ^ f) * 8)) = v2u{ph[hd][1].x, ph[hd][1].y};
        *(LAS v2u*)(size_t)(pa + (((6 + hh) ^ f) * 8)) = v2u{ph[hd][1].z, ph[hd][1].w};
      }
    }
    if constexpr (ATT && i == 17) {
      if (grp == 0) {
        f32x16 kv = zero16();
        f32x4 z4 = {0.f, 0.f, 0.f, 0.f};
        const unsigned gi1 = (lane >> 4) & 1, p4 = lane & 3;
        unsigned pa0 = slot_of(13) + tg * 8192 + (8 * hh + q4) * 64 + (((4 * gi1 + p4) ^ (4 * hh + (q4 >> 1))) * 8);
        unsigned va0 = slot_of(15) + tg * 4096 + (8 * hh + q4) * 32 + ((p4 ^ (2 * hh)) * 8);
        TIE(pa0); TIE(va0);
        const unsigned pa1 = (pa0 ^ 16u) + 256;
        const unsigned va1 = (va0 ^ 8u) + 128;
#pragma unroll
        for (int h2 = 0; h2 < 2; ++h2) {
          v4u pa[4], vb[4];
#pragma unroll
          for (int s = 0; s < 4; ++s) { pa[s] = trfrag(pa0 + (4 * h2 + s) * 1024, pa1 + (4 * h2 + s) * 1024); vb[s] = trfrag(va0 + (4 * h2 + s) * 512, va1 + (4 * h2 + s) * 512); }
          SB();
#pragma unroll
          for (int s = 0; s < 4; ++s) { kv = mfma32(pa[s], vb[s], kv); z4 = mfma16(sel, pa[s], z4); }
          SB();
        }
        const unsigned sKV = (unsigned)WS_KVP + ((4 * cg + tg) * 16 + rg) * 2048, sZ = (unsigned)WS_ZP + ((4 * cg + tg) * 16 + rg) * 128;
        const unsigned vo = (hh * 16 + m) * 16, vz = lane * 4;
        asm volatile("s_nop 15\n\ts_mov_b64 exec, %7\n\t"
                     "buffer_store_dwordx4 %0, %5, %6, %8 offen sc1\n\tbuffer_store_dwordx4 %1, %5, %6, %8 offen offset:512 sc1\n\t"
                     "buffer_store_dwordx4 %2, %5, %6, %8 offen offset:1024 sc1\n\tbuffer_store_dwordx4 %3, %5, %6, %8 offen offset:1536 sc1\n\t"
                     "s_mov_b64 exec, %10\n\tbuffer_store_dword %4, %9, %6, %11 offen sc1\n\ts_mov_b64 exec, -1\n\ts_nop 0"
                     ::"v"(__builtin_shufflevector(kv, kv, 0, 1, 2, 3)), "v"(__builtin_shufflevector(kv, kv, 4, 5, 6, 7)), "v"(__builtin_shufflevector(kv, kv, 8, 9, 10, 11)),
                       "v"(__builtin_shufflevector(kv, kv, 12, 13, 14, 15)), "v"(z4[0]), "v"(vo), "s"(c.rWs), "s"(0x0000FFFF0000FFFFull), "s"(sKV), "v"(vz), "s"(0x00000000FFFFFFFFull), "s"(sZ)
                     : "memory");
      }
    }
    if constexpr (i == 12 || i == 16 || i == 20 || i == 24) STAMP(i / 4 - 1);
  });
  last = acc[ATT ? 0 : 1];
}

template <bool ATT>
DI void stage2(const Params& p, Ctx& c, v4u (&ph)[2][2], v4u (&a2)[4], const f32x16& last, const unsigned tra2) {
  constexpr int N1 = n1_of(ATT);
  const int grp = c.grp, kq = c.tg, th = c.grp;
  int ln = c.lane; TIE(ln); c.lane = ln;
  const int w = c.w;
  const unsigned m = ln & 31, hh = ln >> 5;
  const char* hfsrc = p.ws + WS_HF + (c.rt >> 1) * 256;
  const unsigned sH = (unsigned)WS_ACT + ((2 * c.rt + th) * 144 + 28 * kq) * 1024;
  const unsigned sO = (unsigned)WS_ACT + ((2 * c.rt + th) * 144 + 112 + 8 * kq) * 1024;
  const unsigned sMine = (unsigned)WS_ACT + (c.rg * 4 + c.tg) * 144 * 1024;
  const unsigned l16 = (unsigned)ln * 16;
  const unsigned t8 = (unsigned)(w * 8 + (ln >> 3)), c8 = ln & 7;
  if constexpr (ATT) {
    const char* kfsrc = p.ws + WS_KF + c.cg * 128;
    asm volatile("" : "+s"(kfsrc));
    wait_mailbox(L_MBK, 8, kfsrc, c.tag, ln, c.m16 >> 8);
    STAMP(7);
    v4u part[16], zp;
    const unsigned sK = (unsigned)WS_KVP + ((4 * c.cg + (w >> 1)) * 16) * 2048 + (w & 1) * 1024;
    zp = gload16_sc1((ln >> 2) * 128 + (ln & 3) * 16, p.ws + WS_ZP + (size_t)((4 * c.cg + (w >> 1)) * 16) * 128 + (w & 1) * 64);
    static_for<2>([&](auto R) __attribute__((always_inline)) { constexpr int r = decltype(R)::value; bload16x2s_sc1<r * 4096u>(part[2 * r], part[2 * r + 1], l16, c.rWs, sK); });
    phi_block(last, c, ph, [&](auto K) __attribute__((always_inline)) {
      SB();
      static_for<3>([&](auto R) __attribute__((always_inline)) { constexpr int r = 2 + 3 * decltype(K)::value + decltype(R)::value; bload16x2s_sc1<r * 4096u>(part[2 * r], part[2 * r + 1], l16, c.rWs, sK); });
      SB();
    });
    asm volatile("s_waitcnt vmcnt(0)" : "+v"(part[0]), "+v"(part[1]), "+v"(part[2]), "+v"(part[3]), "+v"(part[4]), "+v"(part[5]), "+v"(part[6]), "+v"(part[7]), "+v"(zp),
                   "+v"(ph[0][0]), "+v"(ph[0][1]), "+v"(ph[1][0]), "+v"(ph[1][1])::"memory");
    asm volatile("" : "+v"(part[8]), "+v"(part[9]), "+v"(part[10]), "+v"(part[11]), "+v"(part[12]), "+v"(part[13]), "+v"(part[14]), "+v"(part[15]));
    f32x4 s4 = __builtin_bit_cast(f32x4, part[0]);
#pragma unroll
    for (int r = 1; r < 16; ++r) s4 += __builtin_bit_cast(f32x4, part[r]);
    const int ch = (w & 1) * 64 + ln, i = ch >> 5, h2 = (ch >> 4) & 1, cc = ch & 15;
    *(LAS v2u*)(size_t)(L_KVA + ((((w >> 1) * 2 + (i >> 1)) * 2 + h2) * 17 + cc) * 16 + (i & 1) * 8) = v2u{pk2(s4[0], s4[1]), pk2(s4[2], s4[3])};
    f32x4 zs = __builtin_bit_cast(f32x4, zp);
#pragma unroll
    for (int k = 0; k < 4; ++k) {
      float v = zs[k];
      v += __uint_as_float(__builtin_amdgcn_update_dpp(0u, __float_as_uint(v), 0x124, 0xf, 0xf, false));
      v += __uint_as_float(__builtin_amdgcn_update_dpp(0u, __float_as_uint(v), 0x128, 0xf, 0xf, false));
      auto r = __builtin_amdgcn_permlane16_swap(__float_as_uint(v), __float_as_uint(v), false, false);
      zs[k] = swap32_add(__uint_as_float(r[0]) + __uint_as_float(r[1]));
    }
    const int zc = (w & 1) * 4 + (ln & 3), zi = zc >> 1, z2 = zc & 1;
    *(LAS v2u*)(size_t)(L_KVA + ((((w >> 1) * 2 + (zi >> 1)) * 2 + z2) * 17 + 16) * 16 + (zi & 1) * 8) = v2u{pk2(zs[0], zs[1]), pk2(zs[2], zs[3])};
    bar();
    const unsigned sL = sMine + (112 + 4 * c.cg + 2 * grp) * 1024;
    static_for<2>([&](auto HD) __attribute__((always_inline)) {
      constexpr int hd = decltype(HD)::value;
      const unsigned ka = L_KVA + (((2 * grp + hd) * 4 + hh) * 17 + (m < 16 ? m : 16)) * 16;
      const v4u k0 = *(LAS v4u*)(size_t)(ka), k1 = *(LAS v4u*)(size_t)(ka + 2 * 17 * 16);
      f32x16 o = mfma32(k0, ph[hd][0], zero16());
      o = mfma32(k1, ph[hd][1], o);
      const float r = __builtin_amdgcn_rcpf(o[8] + 1e-6f);
      bstore16_sc1<hd * 1024u>(v4u{pk2(o[0] * r, o[1] * r), pk2(o[2] * r, o[3] * r), pk2(o[4] * r, o[5] * r), pk2(o[6] * r, o[7] * r)}, l16, c.rWs, sL);
    });
  } else if (grp == 0) {
    const unsigned sL = sMine + (14 * c.cg + ((c.cg & 1) ? 0 : 12)) * 1024;
    static_for<2>([&](auto UU) __attribute__((always_inline)) {
      constexpr int u = decltype(UU)::value;
      float hv[8];
#pragma unroll
      for (int k = 0; k < 8; ++k) hv[k] = silu_b(last[8 * u + k], silu_a(last[8 * u + k], c), c);
      bstore16_sc1<u * 1024u>(v4u{pk2(hv[0], hv[1]), pk2(hv[2], hv[3]), pk2(hv[4], hv[5]), pk2(hv[6], hv[7])}, l16, c.rWs, sL);
    });
  }
  wait_mailbox(L_MBH, 4, hfsrc, c.tag, ln, c.m16 >> 12);
  STAMP(8);
  v4u hb[S2N][2], eflag, xres;
  unsigned sHr[4], sOr[4];
#pragma unroll
  for (int k = 0; k < 4; ++k) { sHr[k] = sH + c.r2h[k] * 2048; sOr[k] = sO + c.r2o[k] * 2048; }
  static_for<4>([&](auto J) __attribute__((always_inline)) { constexpr int j = decltype(J)::value; bload16x2_sc1<0u>(hb[j][0], hb[j][1], l16, c.rWs, sHr[j]); });
  SB();
  f32x16 acc0 = zero16(), acc1 = zero16();
  static_for<S2N>([&](auto J) __attribute__((always_inline)) {
    constexpr int j = decltype(J)::value;
    constexpr int nw = s2_wait(j);
    SB();
    CSTAMP(64 + 2 * j);
    if constexpr (j <= s2_lastbar()) asm volatile("s_waitcnt vmcnt(%2) lgkmcnt(0)\n\ts_barrier" : "+v"(hb[j][0]), "+v"(hb[j][1]) : "i"(nw) : "memory");
    else asm volatile("s_waitcnt vmcnt(%2)" : "+v"(hb[j][0]), "+v"(hb[j][1]) : "i"(nw) : "memory");
    CSTAMP(64 + 2 * j + 1);
    if constexpr (j == 0) flag_store_m(0u, p.ws + WS_HF + c.rg * 256 + (ATT ? 192 : 128) + c.cg * 8, c.tag, c.m1);
    if constexpr (j < S2_O) dma1_sc1_m(l16, hfsrc, L_MBH, c.m16);
    SB();
    {
      unsigned b0 = slot_of(N1 + j + 1) + tra2; TIE(b0);
      const unsigned b1 = b0 ^ 64u;
      v4u an[4];
      acc0 = mfma32(a2[0], hb[j][0], acc0);
      if constexpr (j + 1 < S2N) { an[0] = trfrag(b0, b0 + 1024); an[1] = trfrag(b1, b1 + 1024); }
      SB();
      acc1 = mfma32(a2[1], hb[j][0], acc1);
      if constexpr (j + 1 < S2N) { an[2] = trfrag(b0 + 2048, b0 + 3072); an[3] = trfrag(b1 + 2048, b1 + 3072); }
      SB();
      acc0 = mfma32(a2[2], hb[j][1], acc0);
      acc1 = mfma32(a2[3], hb[j][1], acc1);
      if constexpr (j + 1 < S2N) { a2[0] = an[0]; a2[1] = an[1]; a2[2] = an[2]; a2[3] = an[3]; }
      pin(acc0); pin(acc1);
      SB();
    }
    if constexpr (s2_wat(j) >= 0) issue_s2<(s2_wat(j) >= 0 ? s2_wat(j) : 0), slot_of(N1 + (s2_wat(j) >= 0 ? s2_wat(j) : 0))>(c, c.wdst);
    if constexpr (j == S2_B) { STAMP(9); wait_mailbox(L_MBH + 64, 4, hfsrc + 64, c.tag, ln, c.m16 >> 12); STAMP(10); }
    if constexpr (j == S2_C) { STAMP(11); wait_mailbox(L_MBH + 128, 4, hfsrc + 128, c.tag, ln, c.m16 >> 12); STAMP(12); }
    if constexpr (j == S2_O) { STAMP(13); wait_mailbox(L_MBH + 192, 4, hfsrc + 192, c.tag, ln, c.m16 >> 12); STAMP(14); }
    static_for<s2_cnt(j)>([&](auto TT) __attribute__((always_inline)) {
      constexpr int t = s2_first(j) + decltype(TT)::value;
      if constexpr (t < 12) bload16x2_sc1<(t / 4) * 2 * 2048u>(hb[t][0], hb[t][1], l16, c.rWs, sHr[t & 3]);
      else if constexpr (t < 14) bload16x2_sc1<s2_id(t) * 2048u>(hb[t][0], hb[t][1], l16, c.rWs, sH);
      else bload16x2_sc1<0u>(hb[t][0], hb[t][1], l16, c.rWs, sOr[t - 14]);
    });
    if constexpr (j == 6) xres = gload16(t8 * 1024 + c8 * 16, p.x + (size_t)(64 * c.rt) * 1024 + 128 * c.ct);
    if constexpr (j == 11 && !ATT) { if (blockIdx.x == 0 && w == 1) eflag = gload16_sc1((ln >> 2) * 256 + (ln & 3) * 16, p.ws + WS_HF); }
  });
  if (!ATT && blockIdx.x == 0 && w == 1) {
    asm volatile("s_waitcnt vmcnt(0)" : "+v"(eflag)::"memory");
    for (unsigned spin = 0; spin < SPIN_CAP; ++spin) {
      if (__all(eflag.x == c.tag.x && eflag.y == c.tag.y && eflag.z == c.tag.x && eflag.w == c.tag.y)) break;
      eflag = gload16_sc1((ln >> 2) * 256 + (ln & 3) * 16, p.ws + WS_HF);
      asm volatile("s_waitcnt vmcnt(0)" : "+v"(eflag)::"memory");
    }
    if (ln == 0) gstore8_sc1(0u, p.ws + WS_EPOCH, c.tag);
  }
  CSTAMP(100);
  bar();
  CSTAMP(101);
  STAMP(15);
  {
    const unsigned ra = kq * 17408 + (32 * th + m) * 272 + hh * 16;
#pragma unroll
    for (int i = 0; i < 4; ++i) {
      *(LAS f32x4*)(size_t)(ra + i * 32) = f32x4{acc0[4 * i], acc0[4 * i + 1], acc0[4 * i + 2], acc0[4 * i + 3]};
      *(LAS f32x4*)(size_t)(ra + 128 + i * 32) = f32x4{acc1[4 * i], acc1[4 * i + 1], acc1[4 * i + 2], acc1[4 * i + 3]};
    }
  }
  bar();
  {
    const unsigned ra = t8 * 272 + c8 * 32;
    f32x4 lo = (*(LAS f32x4*)(size_t)(ra) + *(LAS f32x4*)(size_t)(ra + 17408)) + (*(LAS f32x4*)(size_t)(ra + 2 * 17408) + *(LAS f32x4*)(size_t)(ra + 3 * 17408));
    f32x4 hi = (*(LAS f32x4*)(size_t)(ra + 16) + *(LAS f32x4*)(size_t)(ra + 17408 + 16)) + (*(LAS f32x4*)(size_t)(ra + 2 * 17408 + 16) + *(LAS f32x4*)(size_t)(ra + 3 * 17408 + 16));
    TIE(xres);
    const v4u o = v4u{pk2(lo[0] + __uint_as_float(xres.x << 16), lo[1] + __uint_as_float(xres.x & 0xffff0000u)),
                      pk2(lo[2] + __uint_as_float(xres.y << 16), lo[3] + __uint_as_float(xres.y & 0xffff0000u)),
                      pk2(hi[0] + __uint_as_float(xres.z << 16), hi[1] + __uint_as_float(xres.z & 0xffff0000u)),
                      pk2(hi[2] + __uint_as_float(xres.w << 16), hi[3] + __uint_as_float(xres.w & 0xffff0000u))};
    gstore16(t8 * 1024 + c8 * 16, p.out + (size_t)(64 * c.rt) * 1024 + 128 * c.ct, o);
  }
}

__global__ void __launch_bounds__(512, 2) fused(Params p) {
  extern __shared__ __attribute__((aligned(16))) char smem[];
  if ((unsigned)(size_t)smem != 0u) return;
  asm volatile("" ::"s"(p.x), "s"(p.wqkv), "s"(p.wphi), "s"(p.wo), "s"(p.w1), "s"(p.w2), "s"(p.out), "s"(p.ws));
  Ctx c;
  const int tid = threadIdx.x;
  c.lane = tid & 63;
  c.w = __builtin_amdgcn_readfirstlane(tid >> 6);
  c.tg = c.w & 3; c.grp = c.w >> 2;
  const int bid = blockIdx.x, xcd = bid & 7, idx = bid >> 3;
  const int att = idx & 1;
  c.rg = 8 * (xcd & 1) + ((idx >> 1) & 7);
  c.cg = 2 * (xcd >> 1) + (idx >> 4);
  c.rt = 4 * xcd + (idx >> 3); c.ct = idx & 7;
  c.wdst = (unsigned)c.w * 2048;
  c.m16 = (unsigned)__builtin_amdgcn_readfirstlane((tid >> 6) == 4 ? 0xffff : 0); c.m1 = c.m16 >> 15;
  c.vW2a = (c.lane >> 3) * 1024 + ((c.lane & 7) ^ (((c.lane >> 4) & 1) << 2)) * 16;
  c.vW2b = c.vW2a + 8192 - 1024;
  c.s2w0 = (unsigned)(448 * (c.w >> 1) + 16 * (c.w & 1)) * 1024 + 128 * c.ct;
#pragma unroll
  for (int k = 0; k < 4; ++k) {
    const unsigned r = (k + ROT2) & 3;
    c.r2h[k] = (r & 1) + (r >> 1) * 8; c.r2o[k] = r;
    c.s2w[k] = c.s2w0 + c.r2h[k] * 32768;
    c.s2o[k] = (unsigned)(128 * (c.w >> 1) + 16 * (c.w & 1)) * 1024 + 128 * c.ct + r * 32768;
  }
  c.rW2 = make_rsrc(p.w2, 1792 * 1024); c.rWo = make_rsrc(p.wo, 512 * 1024); c.rWs = make_rsrc(p.ws, (unsigned)WS_END);
  STAMP(0);
  const unsigned tra2 = c.tg * 4096 + (4 * (c.lane >> 5) + ((c.lane & 15) >> 2)) * 128 + (((c.lane & 15) >> 3) * 64) + ((c.lane >> 4) & 1) * 32 + (c.lane & 3) * 8;
  v4u ph[2][2], a2[4];
  f32x16 last;
  if (att) { stage1<true>(p, c, ph, a2, last, tra2); STAMP(6); CSTAMP(62); CSTAMP(63); stage2<true>(p, c, ph, a2, last, tra2); }
  else { stage1<false>(p, c, ph, a2, last, tra2); STAMP(6); CSTAMP(62); CSTAMP(63); stage2<false>(p, c, ph, a2, last, tra2); }

  STAMP(16);
  CSTAMP(102);
}
}

extern "C" void kernel_launch(void* const* d_in, const int* in_sizes, int n_in, void* d_out, int out_size, void* d_ws, size_t ws_size,
                              hipStream_t stream) {
  hh::Params p;
  p.x = (const char*)d_in[0]; p.wqkv = (const char*)d_in[1]; p.wphi = (const char*)d_in[2]; p.wo = (const char*)d_in[3];
  p.w1 = (const char*)d_in[5]; p.w2 = (const char*)d_in[6]; p.out = (char*)d_out; p.ws = (char*)d_ws;
  (void)hipFuncSetAttribute((const void*)hh::fused, hipFuncAttributeMaxDynamicSharedMemorySize, hh::LDS_BYTES);
  hh::fused<<<256, 512, hh::LDS_BYTES, stream>>>(p);
}
```

```cpp
#include <hip/hip_runtime.h>
#include <hip/hip_bf16.h>

#define ROT1 ((rg + (ATT ? 2 : 0) + (cg & 1)) & 3)
#define ROT2 ((c.rt + c.ct) & 3)
#define PF_AT 13
#define PF_STEP 1
namespace hh {
#define LAS __attribute__((address_space(3)))
#define GAS __attribute__((address_space(1)))
#define DI __device__ __forceinline__
typedef unsigned v4u __attribute__((ext_vector_type(4)));
typedef unsigned v2u __attribute__((ext_vector_type(2)));
typedef float f32x2 __attribute__((ext_vector_type(2)));
typedef float f32x4 __attribute__((ext_vector_type(4)));
typedef float f32x16 __attribute__((ext_vector_type(16)));
typedef short s16x4 __attribute__((ext_vector_type(4)));
typedef __bf16 bf16x2 __attribute__((ext_vector_type(2)));
typedef __bf16 bf16x8 __attribute__((ext_vector_type(8)));

constexpr int SLOT = 16384, NSLOT = 9;
constexpr int MISC = SLOT * NSLOT;
constexpr int L_ST = MISC;
constexpr int L_CS = L_ST + 2048;
constexpr int L_KVA = L_CS + 4096;
constexpr int L_KVG = MISC;
constexpr int L_PHI = L_KVA + 4352;
constexpr int L_MBH = L_PHI + 2048;
constexpr int L_MBK = L_MBH + 256;
constexpr int LDS_BYTES = 163840;
static_assert(L_MBK + 128 <= LDS_BYTES, "lds");

constexpr size_t WS_HF = 0, WS_KF = 4096, WS_EPOCH = 8192;
constexpr size_t WS_ST = 16384;
constexpr size_t WS_KVP = WS_ST + 2048 * 16;
constexpr size_t WS_ZP = WS_KVP + 32 * 16 * 2048;
constexpr size_t WS_KVG = WS_ZP + 32 * 16 * 128;
constexpr size_t WS_ACT = WS_KVG + 8 * 9216;
constexpr size_t WS_END = WS_ACT + (size_t)64 * 144 * 1024;
constexpr size_t WS_STAMP = 64u << 20;
#define STAMP(k)
#define CSTAMP(k)
struct Params {
  const char* x; const char* wqkv; const char* wphi; const char* wo; const char* w1; const char* w2;
  char* out; char* ws;
};

DI f32x16 mfma32(v4u a, v4u b, f32x16 c) {
  return __builtin_amdgcn_mfma_f32_32x32x16_bf16(__builtin_bit_cast(bf16x8, a), __builtin_bit_cast(bf16x8, b), c, 0, 0, 0);
}
DI f32x4 mfma16(v4u a, v4u b, f32x4 c) {
  return __builtin_amdgcn_mfma_f32_16x16x32_bf16(__builtin_bit_cast(bf16x8, a), __builtin_bit_cast(bf16x8, b), c, 0, 0, 0);
}
DI f32x16 zero16() { f32x16 z;
#pragma unroll
  for (int i = 0; i < 16; ++i) z[i] = 0.f;
  return z; }
DI unsigned pk2(float lo, float hi) { f32x2 v = {lo, hi}; return __builtin_bit_cast(unsigned, __builtin_convertvector(v, bf16x2)); }
DI unsigned pk2v(f32x2 v) { return __builtin_bit_cast(unsigned, __builtin_convertvector(v, bf16x2)); }
DI v2u trr(unsigned addr) { return __builtin_bit_cast(v2u, __builtin_amdgcn_ds_read_tr16_b64_v4i16((LAS s16x4*)(size_t)addr)); }
DI v4u trfrag(unsigned a0, unsigned a1) { v2u lo = trr(a0), hi = trr(a1); return v4u{lo.x, lo.y, hi.x, hi.y}; }
DI float dot2(unsigned a, unsigned b, float c) {
  return __builtin_amdgcn_fdot2_f32_bf16(__builtin_bit_cast(bf16x2, a), __builtin_bit_cast(bf16x2, b), c, false);
}
extern "C" __device__ float hh_fmul_legacy(float, float) __asm("llvm.amdgcn.fmul.legacy");
DI float fmul(float a, float b) { return hh_fmul_legacy(a, b); }
template <int CTRL> DI float dpp_add(float v) { return v + __uint_as_float(__builtin_amdgcn_update_dpp(0u, __float_as_uint(v), CTRL, 0xf, 0xf, false)); }
DI float row_sum16(float v) { return dpp_add<0x128>(dpp_add<0x124>(dpp_add<0x122>(dpp_add<0x121>(v)))); }
DI float lselm(float a, float b, unsigned long long mask) { float r; asm("v_cndmask_b32 %0, %1, %2, %3" : "=v"(r) : "v"(a), "v"(b), "s"(mask)); return r; }
template <unsigned B> DI float lsel(float a, float b) { return lselm(a, b, 0x0101010101010101ull * B); }
DI float swap32_add(float v) {
  auto r = __builtin_amdgcn_permlane32_swap(__float_as_uint(v), __float_as_uint(v), false, false);
  return __uint_as_float(r[0]) + __uint_as_float(r[1]);
}
DI float rows_sum(float v) {
  auto r = __builtin_amdgcn_permlane16_swap(__float_as_uint(v), __float_as_uint(v), false, false);
  return swap32_add(__uint_as_float(r[0]) + __uint_as_float(r[1]));
}
DI float swap32_max(float v) {
  auto r = __builtin_amdgcn_permlane32_swap(__float_as_uint(v), __float_as_uint(v), false, false);
  return fmaxf(__uint_as_float(r[0]), __uint_as_float(r[1]));
}
typedef __amdgpu_buffer_rsrc_t rsrc_t;
DI rsrc_t make_rsrc(const char* base, unsigned bytes) { return __builtin_amdgcn_make_buffer_rsrc((void*)base, 0, bytes, 0x00020000); }
template <unsigned LO, unsigned GO> DI void dma2(unsigned v0, unsigned v1, rsrc_t rs, unsigned sc, unsigned ld) {
  unsigned t;
  asm volatile("s_add_u32 m0, %5, %6\n\ts_add_u32 %0, %4, %7\n\tbuffer_load_dwordx4 %1, %3, %0 offen lds\n\tbuffer_load_dwordx4 %2, %3, %0 offen offset:1024 lds"
               : "=&s"(t) : "v"(v0), "v"(v1), "s"(rs), "s"(sc), "s"(ld), "i"(LO), "i"(GO) : "memory", "scc");
}
DI void dma1_sc1(unsigned v0, const char* s0, unsigned dst) {
  asm volatile("s_mov_b32 m0, %2\n\ts_nop 3\n\tglobal_load_lds_dwordx4 %0, %1 sc1" ::"v"(v0), "s"(s0), "s"(dst) : "memory");
}
DI void dma1_sc1_m(unsigned v0, const char* s0, unsigned dst, unsigned long long mask) {
  asm volatile("s_mov_b32 m0, %2\n\ts_mov_b64 exec, %3\n\tglobal_load_lds_dwordx4 %0, %1 sc1\n\ts_mov_b64 exec, -1" ::"v"(v0), "s"(s0), "s"(dst), "s"(mask) : "memory");
}
DI void dma4_sc1_m(unsigned v0, const char* s0, unsigned dst, unsigned long long mask) {
  asm volatile("s_mov_b32 m0, %2\n\ts_mov_b64 exec, %3\n\tglobal_load_lds_dwordx4 %0, %1 sc1\n\tglobal_load_lds_dwordx4 %0, %1 offset:1024 sc1\n\t"
               "global_load_lds_dwordx4 %0, %1 offset:2048 sc1\n\tglobal_load_lds_dwordx4 %0, %1 offset:3072 sc1\n\ts_mov_b64 exec, -1" ::"v"(v0), "s"(s0), "s"(dst), "s"(mask) : "memory");
}
DI void granules8(v4u (&g)[2][4], unsigned voff, rsrc_t rs, unsigned sc) {
  asm volatile("buffer_load_dwordx4 %0, %8, %9, %10 offen sc1\n\tbuffer_load_dwordx4 %1, %8, %9, %10 offen offset:544 sc1\n\t"
               "buffer_load_dwordx4 %2, %8, %9, %10 offen offset:1088 sc1\n\tbuffer_load_dwordx4 %3, %8, %9, %10 offen offset:1632 sc1\n\t"
               "buffer_load_dwordx4 %4, %8, %9, %10 offen offset:2176 sc1\n\tbuffer_load_dwordx4 %5, %8, %9, %10 offen offset:2720 sc1\n\t"
               "buffer_load_dwordx4 %6, %8, %9, %10 offen offset:3264 sc1\n\tbuffer_load_dwordx4 %7, %8, %9, %10 offen offset:3808 sc1\n\ts_waitcnt vmcnt(0)"
               : "=&v"(g[0][0]), "=&v"(g[0][1]), "=&v"(g[0][2]), "=&v"(g[0][3]), "=&v"(g[1][0]), "=&v"(g[1][1]), "=&v"(g[1][2]), "=&v"(g[1][3])
               : "v"(voff), "s"(rs), "s"(sc) : "memory");
}
DI v4u bload16_sc1(unsigned voff, rsrc_t rs, unsigned sc) {
  v4u r; asm volatile("buffer_load_dwordx4 %0, %1, %2, %3 offen sc1" : "=v"(r) : "v"(voff), "s"(rs), "s"(sc) : "memory"); return r;
}
DI void touch_lines(unsigned v0, const char* s0, unsigned long long mask) {
  asm volatile("s_mov_b32 m0, %3\n\ts_mov_b64 exec, %2\n\tglobal_load_lds_dword %0, %1\n\ts_mov_b64 exec, -1" ::"v"(v0), "s"(s0), "s"(mask), "s"((unsigned)L_KVA) : "memory");
}
DI void flag_store_m(unsigned zero, char* sbase, v2u v, unsigned long long mask) {
  asm volatile("s_mov_b64 exec, %3\n\tglobal_store_dwordx2 %0, %1, %2 sc1\n\ts_mov_b64 exec, -1\n\ts_nop 0" ::"v"(zero), "v"(v), "s"(sbase), "s"(mask) : "memory");
}
template <int N> DI void sync() { asm volatile("s_waitcnt vmcnt(%0) lgkmcnt(0)\n\ts_barrier" ::"i"(N) : "memory"); }
DI void bar() { asm volatile("s_waitcnt lgkmcnt(0)\n\ts_barrier" ::: "memory"); }
DI v4u gload16_sc1(unsigned voff, const char* sbase) {
  v4u r; asm volatile("s_nop 4\n\tglobal_load_dwordx4 %0, %1, %2 sc1" : "=v"(r) : "v"(voff), "s"(sbase) : "memory"); return r;
}
template <unsigned GO> DI void bload16x2_sc1(v4u& a, v4u& b, unsigned voff, rsrc_t rs, unsigned sc) {
  unsigned t;
  asm volatile("s_add_u32 %2, %5, %6\n\tbuffer_load_dwordx4 %0, %3, %4, %2 offen sc1\n\tbuffer_load_dwordx4 %1, %3, %4, %2 offen offset:1024 sc1"
               : "=&v"(a), "=&v"(b), "=&s"(t) : "v"(voff), "s"(rs), "s"(sc), "i"(GO) : "memory", "scc");
}
template <unsigned GO> DI void bload16x2s_sc1(v4u& a, v4u& b, unsigned voff, rsrc_t rs, unsigned sc) {
  unsigned t;
  asm volatile("s_add_u32 %2, %5, %6\n\tbuffer_load_dwordx4 %0, %3, %4, %2 offen sc1\n\tbuffer_load_dwordx4 %1, %3, %4, %2 offen offset:2048 sc1"
               : "=&v"(a), "=&v"(b), "=&s"(t) : "v"(voff), "s"(rs), "s"(sc), "i"(GO) : "memory", "scc");
}
template <unsigned GO> DI void bstore16_sc1(v4u v, unsigned voff, rsrc_t rs, unsigned sc) {
  unsigned t;
  asm volatile("s_add_u32 %0, %4, %5\n\tbuffer_store_dwordx4 %1, %2, %3, %0 offen sc1\n\ts_nop 1" : "=&s"(t) : "v"(v), "v"(voff), "s"(rs), "s"(sc), "i"(GO) : "memory", "scc");
}
DI void bstore16_m_sc1(v4u v, unsigned voff, rsrc_t rs, unsigned sc, unsigned long long mask) {
  asm volatile("s_mov_b64 exec, %4\n\tbuffer_store_dwordx4 %0, %1, %2, %3 offen sc1\n\ts_mov_b64 exec, -1\n\ts_nop 0" ::"v"(v), "v"(voff), "s"(rs), "s"(sc), "s"(mask) : "memory");
}
template <int N> DI void wait_tie(v4u& a, v4u& b) { asm volatile("s_waitcnt vmcnt(%2)" : "+v"(a), "+v"(b) : "i"(N) : "memory"); }
template <int N, bool BAR> DI void sync_tie(v4u& a, v4u& b, v4u& c, v4u& d) {
  if constexpr (BAR) asm volatile("s_waitcnt vmcnt(%4) lgkmcnt(0)\n\ts_barrier" : "+v"(a), "+v"(b), "+v"(c), "+v"(d) : "i"(N) : "memory");
  else asm volatile("s_waitcnt vmcnt(%4)" : "+v"(a), "+v"(b), "+v"(c), "+v"(d) : "i"(N) : "memory");
}
DI v4u gload16(unsigned voff, const char* sbase) {
  v4u r; asm volatile("s_nop 4\n\tglobal_load_dwordx4 %0, %1, %2" : "=v"(r) : "v"(voff), "s"(sbase) : "memory"); return r;
}
DI void gstore16_sc1(unsigned voff, char* sbase, v4u v) {
  asm volatile("s_nop 4\n\tglobal_store_dwordx4 %0, %1, %2 sc1\n\ts_nop 1" ::"v"(voff), "v"(v), "s"(sbase) : "memory");
}
DI void gstore16(unsigned voff, char* sbase, v4u v) {
  asm volatile("s_nop 4\n\tglobal_store_dwordx4 %0, %1, %2\n\ts_nop 1" ::"v"(voff), "v"(v), "s"(sbase) : "memory");
}
DI void gstore8_sc1(unsigned voff, char* sbase, v2u v) {
  asm volatile("s_nop 4\n\tglobal_store_dwordx2 %0, %1, %2 sc1\n\ts_nop 1" ::"v"(voff), "v"(v), "s"(sbase) : "memory");
}
#define TIE(x) asm volatile("" : "+v"(x))
#define SB() __builtin_amdgcn_sched_barrier(0)
DI void pin(f32x16& a) { asm volatile("" : "+v"(a)); }

constexpr float LOG2E = 1.4426950408889634f;
constexpr unsigned ONES2 = 0x3f803f80u;
constexpr int CODE_PF_BYTES = 4 * 7680 + 8192;
constexpr unsigned SPIN_CAP = 1u << 16;

template <int N, class F> DI void static_for(F&& f) {
  if constexpr (N > 0) { static_for<N - 1>(f); f(std::integral_constant<int, N - 1>{}); }
}

DI unsigned long long mb_read(unsigned mb, int nf, int lane) { return *(volatile LAS unsigned long long*)(size_t)(mb + (unsigned)(lane & (nf - 1)) * 8); }
DI void wait_mailbox(unsigned long long f, unsigned mb, int nf, const char* src, v2u tag, int lane, unsigned long long mask) {
  const unsigned long long t = ((unsigned long long)tag.y << 32) | tag.x;
  if (__builtin_expect(__builtin_amdgcn_ballot_w64(f != t) != 0ull, 0)) {
    for (unsigned spin = 0; spin < SPIN_CAP; ++spin) {
      dma1_sc1_m((unsigned)lane * 16, src, mb, mask);
      __builtin_amdgcn_s_sleep(2);
      if (__builtin_amdgcn_ballot_w64(mb_read(mb, nf, lane) != t) == 0ull) break;
    }
  }
  asm volatile("" ::: "memory");
}
#define WAIT_MB(mb, nf, src, mask) wait_mailbox(mb_read(mb, nf, lane), mb, nf, src, c.tag, lane, mask)

DI float max3(float a, float b, float c) { float r; asm("v_max3_f32 %0, %1, %2, %3" : "=v"(r) : "v"(a), "v"(b), "v"(c)); return r; }
DI float max2(float a, float b) { float r; asm("v_max_f32 %0, %1, %2" : "=v"(r) : "v"(a), "v"(b)); return r; }
DI float max8(const f32x16& P, int o) { return max2(max3(P[o], P[o + 1], P[o + 2]), max3(P[o + 3], P[o + 4], max3(P[o + 5], P[o + 6], P[o + 7]))); }
template <bool MAX> DI void cross2(float& a, float& b) {
  auto r = __builtin_amdgcn_permlane32_swap(__float_as_uint(a), __float_as_uint(b), false, false);
  const float t = MAX ? max2(__uint_as_float(r[0]), __uint_as_float(r[1])) : __uint_as_float(r[0]) + __uint_as_float(r[1]);
  unsigned t2 = __float_as_uint(t); asm volatile("s_nop 1" : "+v"(t2));
  auto s = __builtin_amdgcn_permlane32_swap(t2, t2, false, false);
  a = __uint_as_float(s[0]); b = __uint_as_float(s[1]);
}
template <int H, class F> DI void phi_softmax(const f32x16& P, const float sc, v4u& pos, v4u& neg, F&& hook) {
  float mp = max8(P, 0), mn = max8(P, 8);
  asm volatile("s_nop 1" : "+v"(mp), "+v"(mn));
  cross2<true>(mp, mn);
  hook(std::integral_constant<int, 4 * H + 2>{});
  const f32x2 s2 = {sc, sc}, cp = {-mp * sc, -mp * sc}, cn = {-mn * sc, -mn * sc};
  f32x2 e[8];
#pragma unroll
  for (int i = 0; i < 8; ++i) {
    const f32x2 a = __builtin_elementwise_fma(f32x2{P[2 * i], P[2 * i + 1]}, s2, i < 4 ? cp : cn);
    e[i] = f32x2{__builtin_amdgcn_exp2f(a.x), __builtin_amdgcn_exp2f(a.y)};
    if (i == 3) { asm volatile("" : "+v"(e[0]), "+v"(e[1]), "+v"(e[2]), "+v"(e[3])); hook(std::integral_constant<int, 4 * H + 3>{}); }
  }
  asm volatile("" : "+v"(e[4]), "+v"(e[5]), "+v"(e[6]), "+v"(e[7]));
  hook(std::integral_constant<int, 4 * H + 4>{});
  const f32x2 p2 = (e[0] + e[1]) + (e[2] + e[3]), n2 = (e[4] + e[5]) + (e[6] + e[7]);
  float sp = p2.x + p2.y, sn = n2.x + n2.y;
  cross2<false>(sp, sn);
  hook(std::integral_constant<int, 4 * H + 5>{});
  const float rp = __builtin_amdgcn_rcpf(sp), rn = __builtin_amdgcn_rcpf(sn);
  const f32x2 rp2 = {rp, rp}, rn2 = {rn, rn};
  pos = v4u{pk2v(e[0] * rp2), pk2v(e[1] * rp2), pk2v(e[2] * rp2), pk2v(e[3] * rp2)};
  neg = v4u{pk2v(e[4] * rn2), pk2v(e[5] * rn2), pk2v(e[6] * rn2), pk2v(e[7] * rn2)};
}

constexpr bool it_isx(int i) { return i < 12 && (i % 3) != 2; }
constexpr int it_x(int i) { return 2 * (i / 3) + (i % 3); }
constexpr int it_p(int i) { return i < 12 ? 0 : (i - 12) / 4 + 1; }
constexpr int it_q(int i) { return i < 12 ? i / 3 : (i - 12) % 4; }
constexpr int n1_of(bool att) { return att ? 20 : 24; }
constexpr unsigned slot_of(int i) { return (unsigned)(i % NSLOT) * SLOT; }
constexpr int s2_id(int j) { return j < 12 ? 2 * (j / 4) + (j & 1) + ((j & 2) ? 8 : 0) : j < 14 ? j - 6 : j; }
constexpr int S2N = 18;
constexpr int issued_before(bool att, int i) { return i == 12 ? 17 : (att && i >= 15 && i <= 18) ? 21 : (att && i == 19) ? 24 : i + 7; }
constexpr int S2P = 9;
struct Rq { int code, it; };
constexpr Rq SEQ_F[] = {{0, -1}, {1, -1}, {106, -1}, {2, -1}, {3, -1}, {107, -1}, {108, -1},
                        {4, 0}, {5, 0}, {6, 0}, {7, 0},  {109, 1}, {110, 1}, {111, 1}, {112, 1},  {113, 2}, {8, 2}, {9, 2}, {10, 2},
                        {114, 3}, {11, 3}, {12, 3}, {13, 3},  {115, 4}, {116, 4}, {117, 4},  {14, 5}, {15, 5}, {16, 5}, {17, 5}};
constexpr Rq SEQ_A[] = {{0, -1}, {1, -1}, {2, -1}, {3, -1}, {4, -1}, {5, -1}, {6, -1}, {7, -1},
                        {109, 0}, {110, 0}, {8, 0}, {9, 0},  {111, 1}, {112, 1}, {10, 1}, {11, 1},  {113, 2}, {114, 2}, {12, 2}, {13, 2},
                        {115, 3}, {116, 3}, {14, 3}, {15, 3},  {117, 4}, {16, 4}, {17, 4}};
constexpr int seq_n(bool att) { return att ? (int)(sizeof(SEQ_A) / sizeof(Rq)) : (int)(sizeof(SEQ_F) / sizeof(Rq)); }
constexpr Rq seq_at(bool att, int k) { return att ? SEQ_A[k] : SEQ_F[k]; }
constexpr int seq_it(bool att, int code) { for (int k = 0; k < seq_n(att); ++k) if (seq_at(att, k).code == code) return seq_at(att, k).it; return -2; }
constexpr int seq_req(bool att, int J, int r) {
  for (int k = 0; k < seq_n(att); ++k) if (seq_at(att, k).it == J && r-- == 0) return seq_at(att, k).code;
  return -1;
}
constexpr int seq_after(bool att, int code, int J) {
  int n = -1;
  for (int k = 0; k < seq_n(att); ++k) {
    if (n >= 0 && seq_at(att, k).it < J) n += 2;
    if (seq_at(att, k).code == code) n = 0;
  }
  return n < 0 ? 63 : n;
}
constexpr int imin(int a, int b) { return a < b ? a : b; }
constexpr int s2_wait(bool att, int J) {
  return imin(imin(seq_after(att, 2 * J, J), seq_after(att, 2 * J + 1, J)), imin(seq_after(att, 102 + 2 * J, J), seq_after(att, 103 + 2 * J, J)));
}
template <int V> using IC = std::integral_constant<int, V>;

struct Ctx {
  int lane, w, tg, grp, rg, cg, rt, ct;
  unsigned wdst, vW2a, vW2b;
  unsigned s2w[4], s2o[4], s2w0;
  unsigned r2h[4], r2o[4];
  rsrc_t rW2, rWo, rWs;
  v2u tag;
  unsigned long long m16, m1;
  float lna, lni, lnz;
};

template <int J, unsigned LO> DI void issue_s2(const Ctx& c, unsigned ld) {
  if constexpr (J < 12) dma2<LO, (J / 4) * 2 * 32768u>(c.vW2a, c.vW2b, c.rW2, c.s2w[J & 3], ld);
  else if constexpr (J < 14) dma2<LO, s2_id(J) * 32768u>(c.vW2a, c.vW2b, c.rW2, c.s2w0, ld);
  else dma2<LO, 0u>(c.vW2a, c.vW2b, c.rWo, c.s2o[J - 14], ld);
}
DI float silu_a(float a, const Ctx& c) { return __builtin_amdgcn_exp2f(fmul(a, c.lnz)); }
DI float silu_b(float a, float t, const Ctx& c) { return fmul(a, __builtin_amdgcn_rcpf(fmaf(t, c.lni, c.lni))); }
template <class F> DI void phi_block(const f32x16& A, const Ctx& c, v4u (&ph)[2][2], F&& between) {
  const unsigned pa = L_PHI + c.grp * 1024 + (4 * (c.lane >> 5) + ((c.lane & 15) >> 2)) * 32 + (c.lane & 3) * 8;
  const unsigned sg = (c.lane & 16) ? 0x80008000u : 0u;
  f32x16 P[2];
#pragma unroll
  for (int hd = 0; hd < 2; ++hd) {
    const v4u fr = v4u{pk2(A[8 * hd], A[8 * hd + 1]), pk2(A[8 * hd + 2], A[8 * hd + 3]), pk2(A[8 * hd + 4], A[8 * hd + 5]), pk2(A[8 * hd + 6], A[8 * hd + 7])};
    const v4u aphi = trfrag(pa + hd * 512, pa + hd * 512 + 256) ^ sg;
    P[hd] = mfma32(aphi, fr, zero16());
  }
  between(IC<0>{});
  asm volatile("s_nop 15" : "+v"(P[0]), "+v"(P[1]));
  phi_softmax<0>(P[0], -c.lnz, ph[0][0], ph[0][1], between);
  between(IC<1>{});
  phi_softmax<1>(P[1], -c.lnz, ph[1][0], ph[1][1], between);
}

template <bool ATT>
DI void run(const Params& p, Ctx& c) {
  constexpr int N1 = n1_of(ATT);
  constexpr unsigned WS_ = ATT ? 3072 : 3584;
  const int lane = c.lane, w = c.w, tg = c.tg, grp = c.grp, rg = c.rg, cg = c.cg;
  const unsigned m = lane & 31, hh = lane >> 5, q4 = (lane & 15) >> 2;
  const unsigned vXa = (lane >> 3) * 1024 + (((lane & 7) ^ (lane >> 4)) * 16), vXb = (vXa ^ 64u) + 8192 - 1024;
  const unsigned vWa = (lane >> 3) * WS_ + (c.vW2a & 127u), vWb = vWa + 8 * WS_ - 1024;
  const rsrc_t rX = make_rsrc(p.x, 2048 * 1024), rW = make_rsrc(ATT ? p.wqkv : p.w1, 512 * WS_);
  const unsigned rot = ROT1;
  unsigned sX[4], sW[4];
#pragma unroll
  for (int q = 0; q < 4; ++q) {
    sX[q] = (unsigned)(rg * 128 + 16 * w) * 1024 + ((q + rot) & 3) * 256;
    sW[q] = (unsigned)(16 * w) * WS_ + (ATT ? 128 * cg : 448 * cg + ((cg & 1) ? 64 : 0)) + ((q + rot) & 3) * 128 * WS_;
  }
  const unsigned vW3 = (cg & 1) ? -128 : 384;
  const unsigned sAct = (unsigned)WS_ACT + ((rg * 4 + tg) * 144 + 14 * cg + 2 * (grp + (cg & 1))) * 1024;
  const unsigned l16 = lane * 16;

  auto issue = [&](auto I) __attribute__((always_inline)) {
    constexpr int i = decltype(I)::value;
    if constexpr (i < N1) {
      if constexpr (it_isx(i)) dma2<slot_of(i), (it_x(i) & 1) * 128u>(vXa, vXb, rX, sX[it_x(i) >> 1], c.wdst);
      else {
        constexpr int pi = it_p(i), kq = it_q(i);
        constexpr unsigned poff = ATT ? (pi == 0 ? 2048 : pi == 1 ? 1024 : 0) : (pi < 3 ? 128 * pi : 0);
        dma2<slot_of(i), poff>(vWa, vWb, rW, (!ATT && pi == 3) ? sW[kq] + vW3 : sW[kq], c.wdst);
      }
    } else if constexpr (i < N1 + S2N) issue_s2<i - N1, slot_of(i)>(c, c.wdst);
  };
  CSTAMP(14);
  v2u ep;
  asm volatile("s_load_dwordx2 %0, %1, 0x2000" : "=s"(ep) : "s"(p.ws) : "memory");
  static_assert(WS_EPOCH == 0x2000, "epoch offset");
  const unsigned srow = rg * 128 + ((ATT ? 8 : 0) + cg) * 8 + w;
  v4u xr;
  asm volatile("buffer_load_dwordx4 %0, %1, %2, %3 offen" : "=v"(xr) : "v"(l16), "s"(rX), "s"(srow * 1024) : "memory");
  static_for<3>(issue);
  asm volatile("s_barrier" ::: "memory");
  issue(IC<3>{}); issue(IC<4>{}); issue(IC<5>{});
  CSTAMP(15);
  {
    asm volatile("s_waitcnt vmcnt(12) lgkmcnt(0)" : "+v"(xr), "+s"(ep)::"memory");
    c.tag = v2u{ep.x + 1u, ep.y};
    if (threadIdx.x < 24) *(LAS v4u*)(size_t)(L_MBH + threadIdx.x * 16) = v4u{ep.x, 0u, ep.x, 0u};
    float s1 = dot2(xr.y, ONES2, dot2(xr.x, ONES2, 0.f)) + dot2(xr.w, ONES2, dot2(xr.z, ONES2, 0.f));
    float s2 = dot2(xr.y, xr.y, dot2(xr.x, xr.x, 0.f)) + dot2(xr.w, xr.w, dot2(xr.z, xr.z, 0.f));
    s1 = row_sum16(s1); s2 = row_sum16(s2);
    s1 = rows_sum(s1); s2 = rows_sum(s2);
    const float mean = s1 * (1.f / 512.f);
    const float var = fmaxf(s2 * (1.f / 512.f) - mean * mean, 0.f) + 1e-5f;
    const v4u g = {__float_as_uint(mean), c.tag.x, __float_as_uint(var), c.tag.x ^ c.tag.y ^ 0x5bd1e995u};
    asm volatile("s_mov_b64 exec, 1\n\tbuffer_store_dwordx4 %0, %1, %2, %3 offen sc1\n\ts_mov_b64 exec, -1\n\ts_nop 0"
                 ::"v"(g), "v"(l16), "s"(c.rWs), "s"((unsigned)WS_ST + srow * 16) : "memory");
  }

  v4u ph[2][2], a2[8], hb[S2N][2];
  const unsigned tra2 = tg * 4096 + (4 * hh + q4) * 128 + ((q4 >> 1) * 64) + ((lane >> 4) & 1) * 32 + (lane & 3) * 8;
  const char* hfsrc = p.ws + WS_HF + (c.rt >> 1) * 256;
  const unsigned sH = (unsigned)WS_ACT + ((2 * c.rt + grp) * 144 + 28 * tg) * 1024;
  const unsigned sO = (unsigned)WS_ACT + ((2 * c.rt + grp) * 144 + 112 + 8 * tg) * 1024;
  auto hload = [&](auto TT) __attribute__((always_inline)) {
    constexpr int t = decltype(TT)::value;
    if constexpr (t < 12) bload16x2_sc1<(t / 4) * 2 * 2048u>(hb[t][0], hb[t][1], l16, c.rWs, sH + c.r2h[t & 3] * 2048);
    else if constexpr (t < 14) bload16x2_sc1<s2_id(t) * 2048u>(hb[t][0], hb[t][1], l16, c.rWs, sH);
    else bload16x2_sc1<0u>(hb[t][0], hb[t][1], l16, c.rWs, sO + c.r2o[t - 14] * 2048);
  };
  const unsigned tagB = c.tag.x ^ c.tag.y ^ 0x5bd1e995u;

  v4u xf[32], a[4], b[4], c0, c1;
  f32x16 acc[2];
  float hv[8], tv[4];
  f32x4 cs4 = {0.f, 0.f, 0.f, 0.f};
  const unsigned sel1 = ((lane & 31) == 0 || (lane & 31) == 20) ? ONES2 : 0u;
  const v4u sel = {sel1, sel1, sel1, sel1};
  v4u mufrag = {0u, 0u, 0u, 0u};
  const unsigned tok = 32 * tg + m;
  const unsigned xa = tok * 128 + ((hh ^ ((tok >> 1) & 7)) * 16);
  const unsigned trb = (8 * hh + q4) * 128 + ((lane >> 4) & 1) * 32 + (lane & 3) * 8;
  unsigned tra = trb + ((grp ^ (q4 >> 1)) * 64);
  const unsigned csr = L_CS + grp * 1024 + lane * 16;
  const unsigned csw = hh ? (unsigned)L_KVA + m * 8 : L_CS + grp * 1024 + (tg >> 1) * 512 + m * 16 + (tg & 1) * 8;
  unsigned base = 0;

  auto mean_fix = [&](f32x16& A, int par) __attribute__((always_inline)) { A = mfma32(*(LAS v4u*)(size_t)(csr + par * 2048), mufrag, A); };
  auto RC = [&]() __attribute__((always_inline)) { const unsigned cb = base + tg * 4096; c0 = trfrag(cb, cb + 512); c1 = trfrag(cb + 2048, cb + 2560); };
  auto RB = [&](int s) __attribute__((always_inline)) { b[s] = trfrag(base + s * 2048, base + s * 2048 + 512); };
  auto RA = [&](int s) __attribute__((always_inline)) { a[s] = trfrag(base + (4 + s) * 2048, base + (4 + s) * 2048 + 512); };
  auto MU = [&](int up, int uq, int s) __attribute__((always_inline)) { acc[up & 1] = mfma32(a[s], xf[8 * uq + 4 + s], acc[up & 1]); };
  auto MR = [&](int rp, int rq, int s) __attribute__((always_inline)) { acc[rp & 1] = mfma32(b[s], xf[8 * rq + s], (rq == 0 && s == 0) ? zero16() : acc[rp & 1]); };
  auto CS = [&]() __attribute__((always_inline)) { cs4 = mfma16(sel, c0, cs4); cs4 = mfma16(sel, c1, cs4); };
  auto CSW = [&](int rp) __attribute__((always_inline)) {
    const float cs = cs4[0];
    const unsigned hi = pk2(cs, cs);
    const float lo = cs - __uint_as_float(hi << 16);
    *(LAS v2u*)(size_t)(csw + (rp & 1) * 2048) = v2u{hi, pk2(lo, lo)};
    cs4[0] = 0.f;
  };

  const unsigned sSt = (unsigned)WS_ST + (rg * 128 + tg * 32) * 16;
  v4u stg;

  static_for<4>([&](auto Q) __attribute__((always_inline)) {
    constexpr int q = decltype(Q)::value;
    SB();
    CSTAMP(16 + 2 * q);
    sync<6>();
    CSTAMP(16 + 2 * q + 1);
    auto D = [&](auto K) __attribute__((always_inline)) { SB(); issue(IC<3 * q + 6 + decltype(K)::value>{}); SB(); };
    if constexpr (q == 2) stg = bload16_sc1(m * 16, c.rWs, sSt);
    unsigned xb = xa + slot_of(3 * q); TIE(xb);
    base = slot_of(3 * q + 2) + tra; TIE(base);
#pragma unroll
    for (int s = 0; s < 4; ++s) xf[8 * q + s] = *(LAS v4u*)(size_t)(xb ^ (32 * s));
    RB(0); RB(1);
    if constexpr (q >= 1) MU(0, q - 1, 0);
    SB();
    RB(2); RB(3); RC();
    if constexpr (q >= 1) MU(0, q - 1, 1);
    D(IC<0>{});
#pragma unroll
    for (int s = 0; s < 4; ++s) xf[8 * q + 4 + s] = *(LAS v4u*)(size_t)((xb ^ (32 * s)) + SLOT);
    if constexpr (q >= 1) MU(0, q - 1, 2);
    RA(0); SB();
    if constexpr (q >= 1) MU(0, q - 1, 3);
    RA(1); D(IC<1>{});
    RA(2); RA(3); MR(0, q, 0); CS(); SB();
    MR(0, q, 1); D(IC<2>{});
    if constexpr (q == 0 && ATT) { if (w == 7) dma2<L_PHI, 0u>(l16, l16, make_rsrc(p.wphi, 16384), cg * 2048, 0u); }
    MR(0, q, 2);
    if constexpr (q == 3) CSW(0);
    MR(0, q, 3); pin(acc[0]); SB();
    if constexpr (q == 0) STAMP(1);
  });

  static_for<N1 + 1 - 12>([&](auto I) __attribute__((always_inline)) {
    constexpr int i = decltype(I)::value + 12;
    constexpr int nstd = 2 * (issued_before(ATT, i) - i);
    constexpr int nw = (ATT && i == 19) ? 6 : (!ATT && i == 19) ? 4 : (!ATT && i == 23) ? 8 : i == N1 ? nstd - 2 + (ATT ? 0 : 8) : nstd;
    SB();
    CSTAMP(2 * i);
    sync<nw>();
    CSTAMP(2 * i + 1);
    constexpr int up = it_p(i - 1), uq = it_q(i - 1), rp = it_p(i), rq = it_q(i);
    constexpr int ei = i - 13;
    constexpr int eb = ei >= 0 ? ei / 4 : -1, eu = ei >= 0 ? ei % 4 : 0;
    constexpr bool epi = eb >= 0 && (ATT ? eb == 0 : eb < 3);
    constexpr int si = i - 14, sb = si >= 0 ? si / 4 : -1, su = si >= 0 ? si % 4 : 0;
    constexpr bool sto = !ATT && sb >= 0 && sb < 3 && (su & 1);
    constexpr int i0 = i == 12 ? 18 : (ATT && i == 18) ? 22 : (ATT && i == 19) ? 25 : i + 8;
    constexpr int ni = (i == 12 || (ATT && (i == 18 || i == 19))) ? 3 : (ATT && i >= 14 && i <= 17) ? 0 : 1;
    constexpr bool own = true, ownu = true;
    if constexpr (ATT && i == 19) flag_store_m(0u, p.ws + WS_KF + cg * 128 + rg * 8, c.tag, c.m1);
    if constexpr (!ATT && i == 19) flag_store_m(0u, p.ws + WS_HF + rg * 256 + cg * 8, c.tag, c.m1);
    if constexpr (!ATT && i == 23) flag_store_m(0u, p.ws + WS_HF + rg * 256 + 64 + cg * 8, c.tag, c.m1);
    if constexpr (i >= N1 - 4) {
      dma1_sc1_m(l16, hfsrc, L_MBH, c.m16);
      if constexpr (ATT && i == N1) dma1_sc1_m(l16, p.ws + WS_KF + cg * 128, L_MBK, c.m16 >> 8);
    }
    if constexpr (!ATT && i == 22) { WAIT_MB(L_MBH, 8, hfsrc, c.m16 >> 12); STAMP(8); }
    if constexpr (!ATT && (i == 22 || i == 23)) { hload(IC<2 * (i - 22)>{}); hload(IC<2 * (i - 22) + 1>{}); }
    if constexpr (i >= PF_AT && i < PF_AT + 5 * PF_STEP && (i - PF_AT) % PF_STEP == 0) {
      const int n = ((i - PF_AT) / PF_STEP) * 32 + (blockIdx.x >> 3), r = n % 24;
      const char* ps = n < 96 ? p.w2 + (size_t)(448 * (n / 24) + (r < 16 ? 128 + 8 * r : 256 + 8 * r)) * 1024 : p.wo + (size_t)(n - 96) * 8192;
      touch_lines((unsigned)lane * 128u, ps, 0ull - c.m1);
    }
    if constexpr (!ATT && i == 20) tra = trb + (((cg & 1) ^ (q4 >> 1)) * 64);

    auto body = [&](auto UU, auto RR) __attribute__((always_inline)) {
      constexpr bool U = decltype(UU)::value, R = decltype(RR)::value;
      auto D = [&](auto K) __attribute__((always_inline)) {
        constexpr int k = decltype(K)::value;
        if constexpr (ni == 1 ? k == 2 : k < ni) { SB(); issue(IC<i0 + (ni == 1 ? 0 : k)>{}); }
        SB();
      };
      auto VA = [&](int k) __attribute__((always_inline)) {
        if constexpr (epi) {
          if constexpr (ATT) hv[4 * (eu & 1) + k] = fmul(acc[eb & 1][4 * eu + k], c.lna);
          else { tv[k] = silu_a(acc[eb & 1][4 * eu + k], c); if constexpr (eu == 3) { hv[4 + k] = acc[eb & 1][12 + k]; TIE(hv[4 + k]); } }
        }
      };
      auto VB = [&](int k) __attribute__((always_inline)) { if constexpr (epi && !ATT) tv[k] = __builtin_amdgcn_rcpf(fmaf(tv[k], c.lni, c.lni)); };
      auto VC = [&](int k) __attribute__((always_inline)) { if constexpr (epi && !ATT) hv[4 * (eu & 1) + k] = fmul(eu == 3 ? hv[4 + k] : acc[eb & 1][4 * eu + k], tv[k]); };
      if constexpr (R) { base = slot_of(i) + tra; TIE(base); RC(); RB(0); }
      if constexpr (i == 12) {
        v4u g = stg; TIE(g);
        for (unsigned spin = 0; !__all(g.y == c.tag.x && g.w == tagB) && spin < SPIN_CAP; ++spin) {
          g = bload16_sc1(m * 16, c.rWs, sSt);
          asm volatile("s_waitcnt vmcnt(0)" : "+v"(g)::"memory");
        }
        const float mean = __uint_as_float(g.x), var = __uint_as_float(g.z);
        c.lna = __builtin_amdgcn_rsqf(var); c.lni = var * c.lna; c.lnz = -LOG2E * c.lna;
        const float mh = __uint_as_float(pk2(-mean, 0.f) << 16);
        const unsigned mu = pk2(mh, -mean - mh);
        mufrag = v4u{mu, mu, mu, mu};
      }
      if constexpr (U) MU(up, uq, 0);
      if constexpr (sto) bstore16_sc1<(4 * sb + (su >> 1)) * 1024u>(v4u{pk2(hv[0], hv[1]), pk2(hv[2], hv[3]), pk2(hv[4], hv[5]), pk2(hv[6], hv[7])}, l16, c.rWs, sAct);
      D(IC<0>{});
      if constexpr (R) { RB(1); RB(2); }
      if constexpr (U) MU(up, uq, 1);
      VA(0); VA(1); D(IC<1>{});
      if constexpr (R) { RB(3); RA(0); }
      if constexpr (U) MU(up, uq, 2);
      VA(2); VA(3); SB();
      if constexpr (R) RA(1);
      if constexpr (U) MU(up, uq, 3);
      if constexpr (R && own) CS();
      VB(0); VB(1); SB();
      if constexpr (U && uq == 3) mean_fix(acc[up & 1], up & 1);
      if constexpr (R) { RA(2); RA(3); MR(rp, rq, 0); }
      VC(0); VC(1); SB();
      if constexpr (R) MR(rp, rq, 1);
      VB(2); VB(3); D(IC<2>{});
      if constexpr (R) { MR(rp, rq, 2); if constexpr (own && rq == 3) CSW(rp); }
      VC(2); VC(3);
      if constexpr (ATT && epi && (eu & 1)) ph[0][eu >> 1] = v4u{pk2(hv[0], hv[1]), pk2(hv[2], hv[3]), pk2(hv[4], hv[5]), pk2(hv[6], hv[7])};
      SB();
      if constexpr (R) { MR(rp, rq, 3); pin(acc[rp & 1]); }
      SB();
    };
    using T = std::true_type; using F = std::false_type;
    if constexpr (!ATT && i >= 20) {
      if (grp == 0) body(T{}, std::integral_constant<bool, (i < N1)>{});
      else body(std::integral_constant<bool, (up < 3)>{}, F{});
    } else body(T{}, std::integral_constant<bool, (i < N1)>{});
    if constexpr (!ATT && i == N1)
      bstore16_sc1<(4 * 2 + 1) * 1024u>(v4u{pk2(hv[0], hv[1]), pk2(hv[2], hv[3]), pk2(hv[4], hv[5]), pk2(hv[6], hv[7])}, l16, c.rWs, sAct);

    if constexpr (i == N1) {
#pragma unroll
      for (int u = 0; u < 2; ++u) {
        unsigned b0 = slot_of(i + u) + tra2; TIE(b0);
        const unsigned b1 = b0 ^ 64u;
        a2[4 * u] = trfrag(b0, b0 + 1024); a2[4 * u + 1] = trfrag(b1, b1 + 1024); a2[4 * u + 2] = trfrag(b0 + 2048, b0 + 3072); a2[4 * u + 3] = trfrag(b1 + 2048, b1 + 3072);
      }
    }
    if constexpr (ATT && i == 16) {
      {
        const unsigned f = (tok >> 2) & 3;
#pragma unroll
        for (int hd = 0; hd < 2; ++hd) {
          const unsigned va = slot_of(15) + (2 * grp + hd) * 4096 + tok * 32;
          *(LAS v2u*)(size_t)(va + ((hh ^ f) * 8)) = v2u{ph[0][hd].x, ph[0][hd].y};
          *(LAS v2u*)(size_t)(va + (((2 + hh) ^ f) * 8)) = v2u{ph[0][hd].z, ph[0][hd].w};
        }
      }
      phi_block(acc[1], c, ph, [](auto) {});
      const unsigned f = (tok >> 1) & 7;
#pragma unroll
      for (int hd = 0; hd < 2; ++hd) {
        const unsigned pa = slot_of(13) + grp * SLOT + hd * 8192 + tok * 64;
        *(LAS v2u*)(size_t)(pa + (((0 + hh) ^ f) * 8)) = v2u{ph[hd][0].x, ph[hd][0].y};
        *(LAS v2u*)(size_t)(pa + (((2 + hh) ^ f) * 8)) = v2u{ph[hd][0].z, ph[hd][0].w};
        *(LAS v2u*)(size_t)(pa + (((4 + hh) ^ f) * 8)) = v2u{ph[hd][1].x, ph[hd][1].y};
        *(LAS v2u*)(size_t)(pa + (((6 + hh) ^ f) * 8)) = v2u{ph[hd][1].z, ph[hd][1].w};
      }
    }
    if constexpr (ATT && i == 17) {
      if (grp == 0) {
        f32x16 kv = zero16();
        f32x4 z4 = {0.f, 0.f, 0.f, 0.f};
        const unsigned gi1 = (lane >> 4) & 1, p4 = lane & 3;
        unsigned pa0 = slot_of(13) + tg * 8192 + (8 * hh + q4) * 64 + (((4 * gi1 + p4) ^ (4 * hh + (q4 >> 1))) * 8);
        unsigned va0 = slot_of(15) + tg * 4096 + (8 * hh + q4) * 32 + ((p4 ^ (2 * hh)) * 8);
        TIE(pa0); TIE(va0);
        const unsigned pa1 = (pa0 ^ 16u) + 256;
        const unsigned va1 = (va0 ^ 8u) + 128;
#pragma unroll
        for (int h2 = 0; h2 < 2; ++h2) {
          v4u pa[4], vb[4];
#pragma unroll
          for (int s = 0; s < 4; ++s) { pa[s] = trfrag(pa0 + (4 * h2 + s) * 1024, pa1 + (4 * h2 + s) * 1024); vb[s] = trfrag(va0 + (4 * h2 + s) * 512, va1 + (4 * h2 + s) * 512); }
          SB();
#pragma unroll
          for (int s = 0; s < 4; ++s) { kv = mfma32(pa[s], vb[s], kv); z4 = mfma16(sel, pa[s], z4); }
          SB();
        }
        const unsigned sKV = (unsigned)WS_KVP + ((4 * cg + tg) * 16 + rg) * 2048, sZ = (unsigned)WS_ZP + ((4 * cg + tg) * 16 + rg) * 128;
        const unsigned vo = (hh * 16 + m) * 16, vz = lane * 4;
        asm volatile("s_nop 15\n\ts_mov_b64 exec, %7\n\t"
                     "buffer_store_dwordx4 %0, %5, %6, %8 offen sc1\n\tbuffer_store_dwordx4 %1, %5, %6, %8 offen offset:512 sc1\n\t"
                     "buffer_store_dwordx4 %2, %5, %6, %8 offen offset:1024 sc1\n\tbuffer_store_dwordx4 %3, %5, %6, %8 offen offset:1536 sc1\n\t"
                     "s_mov_b64 exec, %10\n\tbuffer_store_dword %4, %9, %6, %11 offen sc1\n\ts_mov_b64 exec, -1\n\ts_nop 0"
                     ::"v"(__builtin_shufflevector(kv, kv, 0, 1, 2, 3)), "v"(__builtin_shufflevector(kv, kv, 4, 5, 6, 7)), "v"(__builtin_shufflevector(kv, kv, 8, 9, 10, 11)),
                       "v"(__builtin_shufflevector(kv, kv, 12, 13, 14, 15)), "v"(z4[0]), "v"(vo), "s"(c.rWs), "s"(0x0000FFFF0000FFFFull), "s"(sKV), "v"(vz), "s"(0x00000000FFFFFFFFull), "s"(sZ)
                     : "memory");
      }
    }
    if constexpr (i == 12 || i == 16 || i == 20 || i == 24) STAMP(i / 4 - 1);
  });
  STAMP(6);
  CSTAMP(62); CSTAMP(63);
  const f32x16 last = acc[ATT ? 0 : 1];
  const unsigned sMine = (unsigned)WS_ACT + (rg * 4 + tg) * 144 * 1024;
  if constexpr (ATT) {
    const char* kfsrc = p.ws + WS_KF + cg * 128;
    asm volatile("" : "+s"(kfsrc));
    WAIT_MB(L_MBK, 16, kfsrc, c.m16 >> 8);
    STAMP(7);
    v4u part = gload16_sc1((2 * w + hh) * 2048 + m * 16, p.ws + WS_KVP + (size_t)((4 * cg + (rg >> 2)) * 16) * 2048 + (rg & 3) * 512);
    v4u zp = gload16_sc1((m >> 1) * 128 + (m & 1) * 16, p.ws + WS_ZP + (size_t)((4 * cg + (rg >> 2)) * 16) * 128 + (rg & 3) * 32);
    WAIT_MB(L_MBH, 8, hfsrc, c.m16 >> 12);
    phi_block(last, c, ph, [&](auto K) __attribute__((always_inline)) {
      if constexpr (decltype(K)::value >= 2) { SB(); hload(IC<(decltype(K)::value >= 2 ? decltype(K)::value - 2 : 0)>{}); SB(); }
      if constexpr (decltype(K)::value == 1) {
        SB();
        wait_tie<8>(part, zp);
        f32x4 s4 = __builtin_bit_cast(f32x4, part);
#pragma unroll
        for (int k = 0; k < 4; ++k) s4[k] = swap32_add(s4[k]);
        *(LAS f32x4*)(size_t)(L_KVA + w * 512 + m * 16) = s4;
        bar();
        if (w == 0) {
          f32x4 t = *(LAS f32x4*)(size_t)(L_KVA + m * 16);
#pragma unroll
          for (int r = 1; r < 8; ++r) t += *(LAS f32x4*)(size_t)(L_KVA + r * 512 + m * 16);
          f32x4 zs = __builtin_bit_cast(f32x4, zp);
#pragma unroll
          for (int k = 0; k < 4; ++k) {
            const float v = dpp_add<0x128>(dpp_add<0x124>(dpp_add<0x122>(zs[k])));
            auto r = __builtin_amdgcn_permlane16_swap(__float_as_uint(v), __float_as_uint(v), false, false);
            const float zt = __uint_as_float(r[0]) + __uint_as_float(r[1]);
            t[k] = hh ? zt : t[k];
          }
          bstore16_m_sc1(v4u{pk2(t[0], t[1]), c.tag.x, pk2(t[2], t[3]), tagB}, l16, c.rWs, (unsigned)WS_KVG + cg * 9216 + rg * 544, 0x3FFFFFFFFull);
        }
        WAIT_MB(L_MBH + 64, 8, hfsrc + 64, c.m16 >> 12);
        SB();
      }
    });
    TIE(ph[1][0]); TIE(ph[1][1]);
    v4u g[2][4];
    const unsigned ga = (m < 16 ? hh * 16 + m : 32 + hh) * 16, gs = (unsigned)WS_KVG + cg * 9216 + grp * 8 * 544;
    for (unsigned spin = 0;; ++spin) {
      granules8(g, ga, c.rWs, gs);
      bool ok = true;
#pragma unroll
      for (int k = 0; k < 8; ++k) ok = ok && g[k >> 2][k & 3].y == c.tag.x && g[k >> 2][k & 3].w == tagB;
      if (__all(ok) || spin > SPIN_CAP) break;
      __builtin_amdgcn_s_sleep(1);
    }
    STAMP(8);
    const unsigned sL = sMine + (112 + 4 * cg + 2 * grp) * 1024;
    static_for<2>([&](auto HD) __attribute__((always_inline)) {
      constexpr int hd = decltype(HD)::value;
      f32x16 o = mfma32(v4u{g[hd][0].x, g[hd][0].z, g[hd][1].x, g[hd][1].z}, ph[hd][0], zero16());
      o = mfma32(v4u{g[hd][2].x, g[hd][2].z, g[hd][3].x, g[hd][3].z}, ph[hd][1], o);
      const float r = __builtin_amdgcn_rcpf(o[8] + 1e-6f);
      bstore16_sc1<hd * 1024u>(v4u{pk2(o[0] * r, o[1] * r), pk2(o[2] * r, o[3] * r), pk2(o[4] * r, o[5] * r), pk2(o[6] * r, o[7] * r)}, l16, c.rWs, sL);
    });
  } else if (grp == 0) {
    const unsigned sL = sMine + (14 * cg + ((cg & 1) ? 0 : 12)) * 1024;
    static_for<2>([&](auto UU) __attribute__((always_inline)) {
      constexpr int u = decltype(UU)::value;
      float h8[8];
#pragma unroll
      for (int k = 0; k < 8; ++k) h8[k] = silu_b(last[8 * u + k], silu_a(last[8 * u + k], c), c);
      bstore16_sc1<u * 1024u>(v4u{pk2(h8[0], h8[1]), pk2(h8[2], h8[3]), pk2(h8[4], h8[5]), pk2(h8[6], h8[7])}, l16, c.rWs, sL);
    });
  }
  const unsigned t8 = (unsigned)(w * 8 + (lane >> 3)), c8 = lane & 7;
  const int kq = tg, th = grp, ln = lane;
  v4u eflag, xres;
  f32x16 acc0 = zero16(), acc1 = zero16();
  static_for<S2P>([&](auto JJ) __attribute__((always_inline)) {
    constexpr int J = decltype(JJ)::value;
    SB();
    CSTAMP(64 + 2 * J);
    sync_tie<s2_wait(ATT, J), (J < S2P - 1)>(hb[2 * J][0], hb[2 * J][1], hb[2 * J + 1][0], hb[2 * J + 1][1]);
    CSTAMP(64 + 2 * J + 1);
    if constexpr (J < seq_it(ATT, 14)) dma1_sc1_m(l16, hfsrc, L_MBH, c.m16);
    if constexpr (J == seq_it(ATT, 4)) { STAMP(9); WAIT_MB(L_MBH + 64, 8, hfsrc + 64, c.m16 >> 12); STAMP(10); }
    if constexpr (J == seq_it(ATT, 8) && J > 0) { STAMP(11); WAIT_MB(L_MBH + 128, 8, hfsrc + 128, c.m16 >> 12); STAMP(12); }
    if constexpr (J == seq_it(ATT, 14)) { STAMP(13); WAIT_MB(L_MBH + 192, 8, hfsrc + 192, c.m16 >> 12); STAMP(14); }
    auto REQ = [&](auto KK) __attribute__((always_inline)) {
      constexpr int code = seq_req(ATT, J, decltype(KK)::value);
      SB();
      if constexpr (code >= 100) issue_s2<(code >= 100 ? code - 100 : 0), slot_of(N1 + (code >= 100 ? code - 100 : 0))>(c, c.wdst);
      else if constexpr (code >= 0) hload(IC<(code >= 0 && code < 100 ? code : 0)>{});
      SB();
    };
    static_assert(seq_req(ATT, J, 4) < 0, "requests per iteration");
    SB();
    {
      v4u an[8];
      static_for<2>([&](auto UU) __attribute__((always_inline)) {
        constexpr int u = decltype(UU)::value;
        unsigned b0 = slot_of(N1 + 2 * J + 2 + u) + tra2; TIE(b0);
        const unsigned b1 = b0 ^ 64u;
        acc0 = mfma32(a2[4 * u], hb[2 * J + u][0], acc0);
        if constexpr (J + 1 < S2P) { an[4 * u] = trfrag(b0, b0 + 1024); an[4 * u + 1] = trfrag(b1, b1 + 1024); }
        acc1 = mfma32(a2[4 * u + 1], hb[2 * J + u][0], acc1);
        REQ(IC<2 * u>{});
        acc0 = mfma32(a2[4 * u + 2], hb[2 * J + u][1], acc0);
        if constexpr (J + 1 < S2P) { an[4 * u + 2] = trfrag(b0 + 2048, b0 + 3072); an[4 * u + 3] = trfrag(b1 + 2048, b1 + 3072); }
        acc1 = mfma32(a2[4 * u + 3], hb[2 * J + u][1], acc1);
        REQ(IC<2 * u + 1>{});
        if constexpr (J == 0 && u == 0) {
          asm volatile("s_waitcnt vmcnt(4)\n\ts_barrier" ::: "memory");
          flag_store_m(0u, p.ws + WS_HF + rg * 256 + (ATT ? 192 : 128) + cg * 8, c.tag, c.m1);
          pin(acc0); pin(acc1);
          if constexpr (seq_it(ATT, 8) == 0) WAIT_MB(L_MBH + 128, 8, hfsrc + 128, c.m16 >> 12);
          SB();
        }
      });
      if constexpr (J + 1 < S2P) {
#pragma unroll
        for (int k = 0; k < 8; ++k) a2[k] = an[k];
      }
      pin(acc0); pin(acc1);
      SB();
    }
    if constexpr (J == 2) xres = gload16(t8 * 1024 + c8 * 16, p.x + (size_t)(64 * c.rt) * 1024 + 128 * c.ct);
    if constexpr (J == 6 && !ATT) { if (blockIdx.x == 0 && w == 1) eflag = gload16_sc1((ln >> 2) * 256 + (ln & 3) * 16, p.ws + WS_HF); }
  });
  if (!ATT && blockIdx.x == 0 && w == 1) {
    asm volatile("s_waitcnt vmcnt(0)" : "+v"(eflag)::"memory");
    for (unsigned spin = 0; spin < SPIN_CAP; ++spin) {
      if (__all(eflag.x == c.tag.x && eflag.y == c.tag.y && eflag.z == c.tag.x && eflag.w == c.tag.y)) break;
      eflag = gload16_sc1((ln >> 2) * 256 + (ln & 3) * 16, p.ws + WS_HF);
      asm volatile("s_waitcnt vmcnt(0)" : "+v"(eflag)::"memory");
    }
    if (ln == 0) gstore8_sc1(0u, p.ws + WS_EPOCH, c.tag);
  }
  CSTAMP(100);
  bar();
  CSTAMP(101);
  STAMP(15);
  {
    const unsigned ra = kq * 17408 + (32 * th + m) * 272 + hh * 16;
#pragma unroll
    for (int i = 0; i < 4; ++i) {
      *(LAS f32x4*)(size_t)(ra + i * 32) = f32x4{acc0[4 * i], acc0[4 * i + 1], acc0[4 * i + 2], acc0[4 * i + 3]};
      *(LAS f32x4*)(size_t)(ra + 128 + i * 32) = f32x4{acc1[4 * i], acc1[4 * i + 1], acc1[4 * i + 2], acc1[4 * i + 3]};
    }
  }
  bar();
  {
    const unsigned ra = t8 * 272 + c8 * 32;
    f32x4 lo = (*(LAS f32x4*)(size_t)(ra) + *(LAS f32x4*)(size_t)(ra + 17408)) + (*(LAS f32x4*)(size_t)(ra + 2 * 17408) + *(LAS f32x4*)(size_t)(ra + 3 * 17408));
    f32x4 hi = (*(LAS f32x4*)(size_t)(ra + 16) + *(LAS f32x4*)(size_t)(ra + 17408 + 16)) + (*(LAS f32x4*)(size_t)(ra + 2 * 17408 + 16) + *(LAS f32x4*)(size_t)(ra + 3 * 17408 + 16));
    TIE(xres);
    const v4u o = v4u{pk2(lo[0] + __uint_as_float(xres.x << 16), lo[1] + __uint_as_float(xres.x & 0xffff0000u)),
                      pk2(lo[2] + __uint_as_float(xres.y << 16), lo[3] + __uint_as_float(xres.y & 0xffff0000u)),
                      pk2(hi[0] + __uint_as_float(xres.z << 16), hi[1] + __uint_as_float(xres.z & 0xffff0000u)),
                      pk2(hi[2] + __uint_as_float(xres.w << 16), hi[3] + __uint_as_float(xres.w & 0xffff0000u))};
    gstore16(t8 * 1024 + c8 * 16, p.out + (size_t)(64 * c.rt) * 1024 + 128 * c.ct, o);
  }
}

__global__ void __launch_bounds__(512, 2) fused(Params p) {
  extern __shared__ __attribute__((aligned(16))) char smem[];
  if ((unsigned)(size_t)smem != 0u) return;
  asm volatile("" ::"s"(p.x), "s"(p.wqkv), "s"(p.wphi), "s"(p.wo), "s"(p.w1), "s"(p.w2), "s"(p.out), "s"(p.ws));
  Ctx c;
  const int tid = threadIdx.x;
  c.lane = tid & 63;
  c.w = __builtin_amdgcn_readfirstlane(tid >> 6);
  c.tg = c.w & 3; c.grp = c.w >> 2;
  const int bid = blockIdx.x, xcd = bid & 7, idx = bid >> 3;
  const int att = idx & 1;
  c.rg = 8 * (xcd & 1) + ((idx >> 1) & 7);
  c.cg = 2 * (xcd >> 1) + (idx >> 4);
  c.rt = 4 * xcd + (idx & 1) + 2 * (idx >> 4); c.ct = (idx >> 1) & 7;
  c.wdst = (unsigned)c.w * 2048;
  c.m16 = (unsigned)__builtin_amdgcn_readfirstlane((tid >> 6) == 4 ? 0xffff : 0); c.m1 = c.m16 >> 15;
  c.vW2a = (c.lane >> 3) * 1024 + ((c.lane & 7) ^ (((c.lane >> 4) & 1) << 2)) * 16;
  c.vW2b = c.vW2a + 8192 - 1024;
  c.s2w0 = (unsigned)(448 * (c.w >> 1) + 16 * (c.w & 1)) * 1024 + 128 * c.ct;
#pragma unroll
  for (int k = 0; k < 4; ++k) {
    const unsigned r = (k + ROT2) & 3;
    c.r2h[k] = (r & 1) + (r >> 1) * 8; c.r2o[k] = r;
    c.s2w[k] = c.s2w0 + c.r2h[k] * 32768;
    c.s2o[k] = (unsigned)(128 * (c.w >> 1) + 16 * (c.w & 1)) * 1024 + 128 * c.ct + r * 32768;
  }
  c.rW2 = make_rsrc(p.w2, 1792 * 1024); c.rWo = make_rsrc(p.wo, 512 * 1024); c.rWs = make_rsrc(p.ws, (unsigned)WS_END);
  STAMP(0);
  {
    const char* pc = (const char*)__builtin_amdgcn_s_getpc();
    asm volatile("s_mov_b32 m0, %3\n\ts_mov_b64 exec, %2\n\tglobal_load_lds_dword %0, %1\n\ts_mov_b64 exec, -1"
                 ::"v"((unsigned)c.lane * 128u + (unsigned)(idx % 5) * 7680u), "s"(pc), "s"(0ull - c.m1), "s"((unsigned)L_KVA) : "memory");
  }
  if (att) run<true>(p, c); else run<false>(p, c);
  STAMP(16);
  CSTAMP(102);
}
}

extern "C" void kernel_launch(void* const* d_in, const int* in_sizes, int n_in, void* d_out, int out_size, void* d_ws, size_t ws_size,
                              hipStream_t stream) {
  hh::Params p;
  p.x = (const char*)d_in[0]; p.wqkv = (const char*)d_in[1]; p.wphi = (const char*)d_in[2]; p.wo = (const char*)d_in[3];
  p.w1 = (const char*)d_in[5]; p.w2 = (const char*)d_in[6]; p.out = (char*)d_out; p.ws = (char*)d_ws;
  (void)hipFuncSetAttribute((const void*)hh::fused, hipFuncAttributeMaxDynamicSharedMemorySize, hh::LDS_BYTES);
  hh::fused<<<256, 512, hh::LDS_BYTES, stream>>>(p);
}
```

```cpp
#include <hip/hip_runtime.h>
#include <hip/hip_bf16.h>

#define ROT1 ((rg + (ATT ? 2 : 0) + (cg & 1)) & 3)
#define ROT2 ((c.rt + c.ct) & 3)
#define PF_AT 13
#define PF_STEP 1
namespace hh {
#define LAS __attribute__((address_space(3)))
#define GAS __attribute__((address_space(1)))
#define DI __device__ __forceinline__
typedef unsigned v4u __attribute__((ext_vector_type(4)));
typedef unsigned v2u __attribute__((ext_vector_type(2)));
typedef float f32x2 __attribute__((ext_vector_type(2)));
typedef float f32x4 __attribute__((ext_vector_type(4)));
typedef float f32x16 __attribute__((ext_vector_type(16)));
typedef short s16x4 __attribute__((ext_vector_type(4)));
typedef __bf16 bf16x2 __attribute__((ext_vector_type(2)));
typedef __bf16 bf16x8 __attribute__((ext_vector_type(8)));

constexpr int SLOT = 16384, NSLOT = 9;
constexpr int MISC = SLOT * NSLOT;
constexpr int L_ST = MISC;
constexpr int L_CS = L_ST + 2048;
constexpr int L_KVA = L_CS + 4096;
constexpr int L_KVG = MISC;
constexpr int L_PHI = L_KVA + 4352;
constexpr int L_MBH = L_PHI + 2048;
constexpr int L_MBK = L_MBH + 256;
constexpr int LDS_BYTES = 163840;
static_assert(L_MBK + 128 <= LDS_BYTES, "lds");

constexpr size_t WS_HF = 0, WS_KF = 4096, WS_EPOCH = 8192;
constexpr size_t WS_ST = 16384;
constexpr size_t WS_KVP = WS_ST + 2048 * 16;
constexpr size_t WS_ZP = WS_KVP + 32 * 16 * 2048;
constexpr size_t WS_KVG = WS_ZP + 32 * 16 * 128;
constexpr size_t WS_ACT = WS_KVG + 8 * 9216;
constexpr size_t WS_END = WS_ACT + (size_t)64 * 144 * 1024;
constexpr size_t WS_STAMP = 64u << 20;
#define STAMP(k)
#define CSTAMP(k)
struct Params {
  const char* x; const char* wqkv; const char* wphi; const char* wo; const char* w1; const char* w2;
  char* out; char* ws;
};

DI f32x16 mfma32(v4u a, v4u b, f32x16 c) {
  return __builtin_amdgcn_mfma_f32_32x32x16_bf16(__builtin_bit_cast(bf16x8, a), __builtin_bit_cast(bf16x8, b), c, 0, 0, 0);
}
DI f32x4 mfma16(v4u a, v4u b, f32x4 c) {
  return __builtin_amdgcn_mfma_f32_16x16x32_bf16(__builtin_bit_cast(bf16x8, a), __builtin_bit_cast(bf16x8, b), c, 0, 0, 0);
}
DI f32x16 zero16() { f32x16 z;
#pragma unroll
  for (int i = 0; i < 16; ++i) z[i] = 0.f;
  return z; }
DI unsigned pk2(float lo, float hi) { f32x2 v = {lo, hi}; return __builtin_bit_cast(unsigned, __builtin_convertvector(v, bf16x2)); }
DI unsigned pk2v(f32x2 v) { return __builtin_bit_cast(unsigned, __builtin_convertvector(v, bf16x2)); }
DI v2u trr(unsigned addr) { return __builtin_bit_cast(v2u, __builtin_amdgcn_ds_read_tr16_b64_v4i16((LAS s16x4*)(size_t)addr)); }
DI v4u trfrag(unsigned a0, unsigned a1) { v2u lo = trr(a0), hi = trr(a1); return v4u{lo.x, lo.y, hi.x, hi.y}; }
DI float dot2(unsigned a, unsigned b, float c) {
  return __builtin_amdgcn_fdot2_f32_bf16(__builtin_bit_cast(bf16x2, a), __builtin_bit_cast(bf16x2, b), c, false);
}
extern "C" __device__ float hh_fmul_legacy(float, float) __asm("llvm.amdgcn.fmul.legacy");
DI float fmul(float a, float b) { return hh_fmul_legacy(a, b); }
template <int CTRL> DI float dpp_add(float v) { return v + __uint_as_float(__builtin_amdgcn_update_dpp(0u, __float_as_uint(v), CTRL, 0xf, 0xf, false)); }
DI float row_sum16(float v) { return dpp_add<0x128>(dpp_add<0x124>(dpp_add<0x122>(dpp_add<0x121>(v)))); }
DI float lselm(float a, float b, unsigned long long mask) { float r; asm("v_cndmask_b32 %0, %1, %2, %3" : "=v"(r) : "v"(a), "v"(b), "s"(mask)); return r; }
template <unsigned B> DI float lsel(float a, float b) { return lselm(a, b, 0x0101010101010101ull * B); }
DI float swap32_add(float v) {
  auto r = __builtin_amdgcn_permlane32_swap(__float_as_uint(v), __float_as_uint(v), false, false);
  return __uint_as_float(r[0]) + __uint_as_float(r[1]);
}
DI float rows_sum(float v) {
  auto r = __builtin_amdgcn_permlane16_swap(__float_as_uint(v), __float_as_uint(v), false, false);
  return swap32_add(__uint_as_float(r[0]) + __uint_as_float(r[1]));
}
DI float swap32_max(float v) {
  auto r = __builtin_amdgcn_permlane32_swap(__float_as_uint(v), __float_as_uint(v), false, false);
  return fmaxf(__uint_as_float(r[0]), __uint_as_float(r[1]));
}
typedef __amdgpu_buffer_rsrc_t rsrc_t;
DI rsrc_t make_rsrc(const char* base, unsigned bytes) { return __builtin_amdgcn_make_buffer_rsrc((void*)base, 0, bytes, 0x00020000); }
template <unsigned LO, unsigned GO> DI void dma2(unsigned v0, unsigned v1, rsrc_t rs, unsigned sc, unsigned ld) {
  unsigned t;
  asm volatile("s_add_u32 m0, %5, %6\n\ts_add_u32 %0, %4, %7\n\tbuffer_load_dwordx4 %1, %3, %0 offen lds\n\tbuffer_load_dwordx4 %2, %3, %0 offen offset:1024 lds"
               : "=&s"(t) : "v"(v0), "v"(v1), "s"(rs), "s"(sc), "s"(ld), "i"(LO), "i"(GO) : "memory", "scc");
}
DI void dma1_sc1(unsigned v0, const char* s0, unsigned dst) {
  asm volatile("s_mov_b32 m0, %2\n\ts_nop 3\n\tglobal_load_lds_dwordx4 %0, %1 sc1" ::"v"(v0), "s"(s0), "s"(dst) : "memory");
}
DI void dma1_sc1_m(unsigned v0, const char* s0, unsigned dst, unsigned long long mask) {
  asm volatile("s_mov_b32 m0, %2\n\ts_mov_b64 exec, %3\n\tglobal_load_lds_dwordx4 %0, %1 sc1\n\ts_mov_b64 exec, -1" ::"v"(v0), "s"(s0), "s"(dst), "s"(mask) : "memory");
}
DI void dma4_sc1_m(unsigned v0, const char* s0, unsigned dst, unsigned long long mask) {
  asm volatile("s_mov_b32 m0, %2\n\ts_mov_b64 exec, %3\n\tglobal_load_lds_dwordx4 %0, %1 sc1\n\tglobal_load_lds_dwordx4 %0, %1 offset:1024 sc1\n\t"
               "global_load_lds_dwordx4 %0, %1 offset:2048 sc1\n\tglobal_load_lds_dwordx4 %0, %1 offset:3072 sc1\n\ts_mov_b64 exec, -1" ::"v"(v0), "s"(s0), "s"(dst), "s"(mask) : "memory");
}
DI void granules8(v4u (&g)[2][4], unsigned voff, rsrc_t rs, unsigned sc) {
  asm volatile("buffer_load_dwordx4 %0, %8, %9, %10 offen sc1\n\tbuffer_load_dwordx4 %1, %8, %9, %10 offen offset:544 sc1\n\t"
               "buffer_load_dwordx4 %2, %8, %9, %10 offen offset:1088 sc1\n\tbuffer_load_dwordx4 %3, %8, %9, %10 offen offset:1632 sc1\n\t"
               "buffer_load_dwordx4 %4, %8, %9, %10 offen offset:2176 sc1\n\tbuffer_load_dwordx4 %5, %8, %9, %10 offen offset:2720 sc1\n\t"
               "buffer_load_dwordx4 %6, %8, %9, %10 offen offset:3264 sc1\n\tbuffer_load_dwordx4 %7, %8, %9, %10 offen offset:3808 sc1\n\ts_waitcnt vmcnt(0)"
               : "=&v"(g[0][0]), "=&v"(g[0][1]), "=&v"(g[0][2]), "=&v"(g[0][3]), "=&v"(g[1][0]), "=&v"(g[1][1]), "=&v"(g[1][2]), "=&v"(g[1][3])
               : "v"(voff), "s"(rs), "s"(sc) : "memory");
}
DI v4u bload16_sc1(unsigned voff, rsrc_t rs, unsigned sc) {
  v4u r; asm volatile("buffer_load_dwordx4 %0, %1, %2, %3 offen sc1" : "=v"(r) : "v"(voff), "s"(rs), "s"(sc) : "memory"); return r;
}
DI void touch_lines(unsigned v0, const char* s0, unsigned long long mask) {
  asm volatile("s_mov_b32 m0, %3\n\ts_mov_b64 exec, %2\n\tglobal_load_lds_dword %0, %1\n\ts_mov_b64 exec, -1" ::"v"(v0), "s"(s0), "s"(mask), "s"((unsigned)L_KVA) : "memory");
}
DI void flag_store_m(unsigned zero, char* sbase, v2u v, unsigned long long mask) {
  asm volatile("s_mov_b64 exec, %3\n\tglobal_store_dwordx2 %0, %1, %2 sc1\n\ts_mov_b64 exec, -1\n\ts_nop 0" ::"v"(zero), "v"(v), "s"(sbase), "s"(mask) : "memory");
}
template <int N> DI void sync() { asm volatile("s_waitcnt vmcnt(%0) lgkmcnt(0)\n\ts_barrier" ::"i"(N) : "memory"); }
DI void bar() { asm volatile("s_waitcnt lgkmcnt(0)\n\ts_barrier" ::: "memory"); }
DI v4u gload16_sc1(unsigned voff, const char* sbase) {
  v4u r; asm volatile("s_nop 4\n\tglobal_load_dwordx4 %0, %1, %2 sc1" : "=v"(r) : "v"(voff), "s"(sbase) : "memory"); return r;
}
template <unsigned GO> DI void bload16x2_sc1(v4u& a, v4u& b, unsigned voff, rsrc_t rs, unsigned sc) {
  unsigned t;
  asm volatile("s_add_u32 %2, %5, %6\n\tbuffer_load_dwordx4 %0, %3, %4, %2 offen sc1\n\tbuffer_load_dwordx4 %1, %3, %4, %2 offen offset:1024 sc1"
               : "=&v"(a), "=&v"(b), "=&s"(t) : "v"(voff), "s"(rs), "s"(sc), "i"(GO) : "memory", "scc");
}
template <unsigned GO> DI void bload16x2s_sc1(v4u& a, v4u& b, unsigned voff, rsrc_t rs, unsigned sc) {
  unsigned t;
  asm volatile("s_add_u32 %2, %5, %6\n\tbuffer_load_dwordx4 %0, %3, %4, %2 offen sc1\n\tbuffer_load_dwordx4 %1, %3, %4, %2 offen offset:2048 sc1"
               : "=&v"(a), "=&v"(b), "=&s"(t) : "v"(voff), "s"(rs), "s"(sc), "i"(GO) : "memory", "scc");
}
template <unsigned GO> DI void bstore16_sc1(v4u v, unsigned voff, rsrc_t rs, unsigned sc) {
  unsigned t;
  asm volatile("s_add_u32 %0, %4, %5\n\tbuffer_store_dwordx4 %1, %2, %3, %0 offen sc1\n\ts_nop 1" : "=&s"(t) : "v"(v), "v"(voff), "s"(rs), "s"(sc), "i"(GO) : "memory", "scc");
}
DI void bstore16_m_sc1(v4u v, unsigned voff, rsrc_t rs, unsigned sc, unsigned long long mask) {
  asm volatile("s_mov_b64 exec, %4\n\tbuffer_store_dwordx4 %0, %1, %2, %3 offen sc1\n\ts_mov_b64 exec, -1\n\ts_nop 0" ::"v"(v), "v"(voff), "s"(rs), "s"(sc), "s"(mask) : "memory");
}
template <int N> DI void wait_tie(v4u& a, v4u& b) { asm volatile("s_waitcnt vmcnt(%2)" : "+v"(a), "+v"(b) : "i"(N) : "memory"); }
template <int N, bool BAR> DI void sync_tie(v4u& a, v4u& b, v4u& c, v4u& d) {
  if constexpr (BAR) asm volatile("s_waitcnt vmcnt(%4) lgkmcnt(0)\n\ts_barrier" : "+v"(a), "+v"(b), "+v"(c), "+v"(d) : "i"(N) : "memory");
  else asm volatile("s_waitcnt vmcnt(%4)" : "+v"(a), "+v"(b), "+v"(c), "+v"(d) : "i"(N) : "memory");
}
DI v4u gload16(unsigned voff, const char* sbase) {
  v4u r; asm volatile("s_nop 4\n\tglobal_load_dwordx4 %0, %1, %2" : "=v"(r) : "v"(voff), "s"(sbase) : "memory"); return r;
}
DI void gstore16_sc1(unsigned voff, char* sbase, v4u v) {
  asm volatile("s_nop 4\n\tglobal_store_dwordx4 %0, %1, %2 sc1\n\ts_nop 1" ::"v"(voff), "v"(v), "s"(sbase) : "memory");
}
DI void gstore16(unsigned voff, char* sbase, v4u v) {
  asm volatile("s_nop 4\n\tglobal_store_dwordx4 %0, %1, %2 sc1\n\ts_nop 1" ::"v"(voff), "v"(v), "s"(sbase) : "memory");
}
DI void gstore8_sc1(unsigned voff, char* sbase, v2u v) {
  asm volatile("s_nop 4\n\tglobal_store_dwordx2 %0, %1, %2 sc1\n\ts_nop 1" ::"v"(voff), "v"(v), "s"(sbase) : "memory");
}
#define TIE(x) asm volatile("" : "+v"(x))
#define SB() __builtin_amdgcn_sched_barrier(0)
DI void pin(f32x16& a) { asm volatile("" : "+v"(a)); }

constexpr float LOG2E = 1.4426950408889634f;
constexpr unsigned ONES2 = 0x3f803f80u;
constexpr unsigned SPIN_CAP = 1u << 16;

template <int N, class F> DI void static_for(F&& f) {
  if constexpr (N > 0) { static_for<N - 1>(f); f(std::integral_constant<int, N - 1>{}); }
}

DI unsigned long long mb_read(unsigned mb, int nf, int lane) { return *(volatile LAS unsigned long long*)(size_t)(mb + (unsigned)(lane & (nf - 1)) * 8); }
DI void wait_mailbox(unsigned long long f, unsigned mb, int nf, const char* src, v2u tag, int lane, unsigned long long mask) {
  const unsigned long long t = ((unsigned long long)tag.y << 32) | tag.x;
  if (__builtin_expect(__builtin_amdgcn_ballot_w64(f != t) != 0ull, 0)) {
    for (unsigned spin = 0; spin < SPIN_CAP; ++spin) {
      dma1_sc1_m((unsigned)lane * 16, src, mb, mask);
      __builtin_amdgcn_s_sleep(2);
      if (__builtin_amdgcn_ballot_w64(mb_read(mb, nf, lane) != t) == 0ull) break;
    }
  }
  asm volatile("" ::: "memory");
}
#define WAIT_MB(mb, nf, src, mask) wait_mailbox(mb_read(mb, nf, lane), mb, nf, src, c.tag, lane, mask)

DI float max3(float a, float b, float c) { float r; asm("v_max3_f32 %0, %1, %2, %3" : "=v"(r) : "v"(a), "v"(b), "v"(c)); return r; }
DI float max2(float a, float b) { float r; asm("v_max_f32 %0, %1, %2" : "=v"(r) : "v"(a), "v"(b)); return r; }
DI float max8(const f32x16& P, int o) { return max2(max3(P[o], P[o + 1], P[o + 2]), max3(P[o + 3], P[o + 4], max3(P[o + 5], P[o + 6], P[o + 7]))); }
template <bool MAX> DI void cross2(float& a, float& b) {
  auto r = __builtin_amdgcn_permlane32_swap(__float_as_uint(a), __float_as_uint(b), false, false);
  const float t = MAX ? max2(__uint_as_float(r[0]), __uint_as_float(r[1])) : __uint_as_float(r[0]) + __uint_as_float(r[1]);
  unsigned t2 = __float_as_uint(t); asm volatile("s_nop 1" : "+v"(t2));
  auto s = __builtin_amdgcn_permlane32_swap(t2, t2, false, false);
  a = __uint_as_float(s[0]); b = __uint_as_float(s[1]);
}
template <int H, class F> DI void phi_softmax(const f32x16& P, const float sc, v4u& pos, v4u& neg, F&& hook) {
  float mp = max8(P, 0), mn = max8(P, 8);
  asm volatile("s_nop 1" : "+v"(mp), "+v"(mn));
  cross2<true>(mp, mn);
  hook(std::integral_constant<int, 4 * H + 2>{});
  const f32x2 s2 = {sc, sc}, cp = {-mp * sc, -mp * sc}, cn = {-mn * sc, -mn * sc};
  f32x2 e[8];
#pragma unroll
  for (int i = 0; i < 8; ++i) {
    const f32x2 a = __builtin_elementwise_fma(f32x2{P[2 * i], P[2 * i + 1]}, s2, i < 4 ? cp : cn);
    e[i] = f32x2{__builtin_amdgcn_exp2f(a.x), __builtin_amdgcn_exp2f(a.y)};
    if (i == 3) { asm volatile("" : "+v"(e[0]), "+v"(e[1]), "+v"(e[2]), "+v"(e[3])); hook(std::integral_constant<int, 4 * H + 3>{}); }
  }
  asm volatile("" : "+v"(e[4]), "+v"(e[5]), "+v"(e[6]), "+v"(e[7]));
  hook(std::integral_constant<int, 4 * H + 4>{});
  const f32x2 p2 = (e[0] + e[1]) + (e[2] + e[3]), n2 = (e[4] + e[5]) + (e[6] + e[7]);
  float sp = p2.x + p2.y, sn = n2.x + n2.y;
  cross2<false>(sp, sn);
  hook(std::integral_constant<int, 4 * H + 5>{});
  const float rp = __builtin_amdgcn_rcpf(sp), rn = __builtin_amdgcn_rcpf(sn);
  const f32x2 rp2 = {rp, rp}, rn2 = {rn, rn};
  pos = v4u{pk2v(e[0] * rp2), pk2v(e[1] * rp2), pk2v(e[2] * rp2), pk2v(e[3] * rp2)};
  neg = v4u{pk2v(e[4] * rn2), pk2v(e[5] * rn2), pk2v(e[6] * rn2), pk2v(e[7] * rn2)};
}

constexpr bool it_isx(int i) { return i < 12 && (i % 3) != 2; }
constexpr int it_x(int i) { return 2 * (i / 3) + (i % 3); }
constexpr int it_p(int i) { return i < 12 ? 0 : (i - 12) / 4 + 1; }
constexpr int it_q(int i) { return i < 12 ? i / 3 : (i - 12) % 4; }
constexpr int n1_of(bool att) { return att ? 20 : 24; }
constexpr unsigned slot_of(int i) { return (unsigned)(i % NSLOT) * SLOT; }
constexpr int s2_id(int j) { return j < 12 ? 2 * (j / 4) + (j & 1) + ((j & 2) ? 8 : 0) : j < 14 ? j - 6 : j; }
constexpr int S2N = 18;
constexpr int s1_ni(bool att, int i) {
  if (i == 12 || i == n1_of(att)) return 2;
  if (att) return (i >= 15 && i <= 17) ? 0 : i == 18 ? 2 : i == 19 ? 3 : 1;
  return 1;
}
constexpr int s1_i0(bool att, int i) { int n = 18; for (int k = 12; k < i; ++k) n += s1_ni(att, k); return n; }
static_assert(s1_i0(true, 21) == 29 && s1_i0(false, 25) == 33, "the first nine stage-2 W items are requested in stage 1");
constexpr bool s1_sync(bool att, int i) { return (i & 1) == 0 || (att && i == 17) || (!att && (i == 19 || i == 23)); }
constexpr int S2P = 9;
struct Rq { int code, it; };
constexpr Rq SEQ_F[] = {{0, -1}, {1, -1}, {105, -1}, {2, -1}, {3, -1}, {106, -1}, {107, -1}, {108, -1},
                        {4, 0}, {5, 0}, {6, 0}, {7, 0},  {109, 1}, {110, 1}, {111, 1}, {112, 1},  {113, 2}, {8, 2}, {9, 2}, {10, 2},
                        {114, 3}, {11, 3}, {12, 3}, {13, 3},  {115, 4}, {116, 4}, {117, 4},  {14, 5}, {15, 5}, {16, 5}, {17, 5}};
constexpr Rq SEQ_A[] = {{0, -1}, {1, -1}, {2, -1}, {3, -1}, {4, -1}, {5, -1}, {6, -1}, {7, -1},
                        {109, 0}, {110, 0}, {8, 0}, {9, 0},  {111, 1}, {112, 1}, {10, 1}, {11, 1},  {113, 2}, {114, 2}, {12, 2}, {13, 2},
                        {115, 3}, {116, 3}, {14, 3}, {15, 3},  {117, 4}, {16, 4}, {17, 4}};
constexpr int seq_n(bool att) { return att ? (int)(sizeof(SEQ_A) / sizeof(Rq)) : (int)(sizeof(SEQ_F) / sizeof(Rq)); }
constexpr Rq seq_at(bool att, int k) { return att ? SEQ_A[k] : SEQ_F[k]; }
constexpr int seq_it(bool att, int code) { for (int k = 0; k < seq_n(att); ++k) if (seq_at(att, k).code == code) return seq_at(att, k).it; return -2; }
constexpr int seq_req(bool att, int J, int r) {
  for (int k = 0; k < seq_n(att); ++k) if (seq_at(att, k).it == J && r-- == 0) return seq_at(att, k).code;
  return -1;
}
constexpr int seq_after(bool att, int code, int J) {
  int n = -1;
  for (int k = 0; k < seq_n(att); ++k) {
    if (n >= 0 && seq_at(att, k).it < J) n += 2;
    if (seq_at(att, k).code == code) n = 0;
  }
  return n < 0 ? 63 : n;
}
constexpr int imin(int a, int b) { return a < b ? a : b; }
constexpr int s2_wait(bool att, int J) {
  return imin(imin(seq_after(att, 2 * J, J), seq_after(att, 2 * J + 1, J)), imin(seq_after(att, 102 + 2 * J, J), seq_after(att, 103 + 2 * J, J)));
}
template <int V> using IC = std::integral_constant<int, V>;

struct Ctx {
  int lane, w, tg, grp, rg, cg, rt, ct;
  unsigned wdst, vW2a, vW2b;
  unsigned s2w[4], s2o[4], s2w0;
  unsigned r2h[4], r2o[4];
  rsrc_t rW2, rWo, rWs;
  v2u tag;
  unsigned long long m16, m1;
  float lna, lni, lnz;
};

template <int J, unsigned LO> DI void issue_s2(const Ctx& c, unsigned ld) {
  if constexpr (J < 12) dma2<LO, (J / 4) * 2 * 32768u>(c.vW2a, c.vW2b, c.rW2, c.s2w[J & 3], ld);
  else if constexpr (J < 14) dma2<LO, s2_id(J) * 32768u>(c.vW2a, c.vW2b, c.rW2, c.s2w0, ld);
  else dma2<LO, 0u>(c.vW2a, c.vW2b, c.rWo, c.s2o[J - 14], ld);
}
DI float silu_a(float a, const Ctx& c) { return __builtin_amdgcn_exp2f(fmul(a, c.lnz)); }
DI float silu_b(float a, float t, const Ctx& c) { return fmul(a, __builtin_amdgcn_rcpf(fmaf(t, c.lni, c.lni))); }
template <class F> DI void phi_block(const f32x16& A, const Ctx& c, v4u (&ph)[2][2], F&& between) {
  const unsigned pa = L_PHI + c.grp * 1024 + (4 * (c.lane >> 5) + ((c.lane & 15) >> 2)) * 32 + (c.lane & 3) * 8;
  const unsigned sg = (c.lane & 16) ? 0x80008000u : 0u;
  f32x16 P[2];
#pragma unroll
  for (int hd = 0; hd < 2; ++hd) {
    const v4u fr = v4u{pk2(A[8 * hd], A[8 * hd + 1]), pk2(A[8 * hd + 2], A[8 * hd + 3]), pk2(A[8 * hd + 4], A[8 * hd + 5]), pk2(A[8 * hd + 6], A[8 * hd + 7])};
    const v4u aphi = trfrag(pa + hd * 512, pa + hd * 512 + 256) ^ sg;
    P[hd] = mfma32(aphi, fr, zero16());
  }
  between(IC<0>{});
  asm volatile("s_nop 15" : "+v"(P[0]), "+v"(P[1]));
  phi_softmax<0>(P[0], -c.lnz, ph[0][0], ph[0][1], between);
  between(IC<1>{});
  phi_softmax<1>(P[1], -c.lnz, ph[1][0], ph[1][1], between);
}

template <bool ATT>
DI void run(const Params& p, Ctx& c) {
  constexpr int N1 = n1_of(ATT);
  constexpr unsigned WS_ = ATT ? 3072 : 3584;
  const int lane = c.lane, w = c.w, tg = c.tg, grp = c.grp, rg = c.rg, cg = c.cg;
  const unsigned m = lane & 31, hh = lane >> 5, q4 = (lane & 15) >> 2;
  const unsigned vXa = (lane >> 3) * 1024 + (((lane & 7) ^ (lane >> 4)) * 16), vXb = (vXa ^ 64u) + 8192 - 1024;
  const unsigned vWa = (lane >> 3) * WS_ + (c.vW2a & 127u), vWb = vWa + 8 * WS_ - 1024;
  const rsrc_t rX = make_rsrc(p.x, 2048 * 1024), rW = make_rsrc(ATT ? p.wqkv : p.w1, 512 * WS_);
  const unsigned rot = ROT1;
  unsigned sX[4], sW[4];
#pragma unroll
  for (int q = 0; q < 4; ++q) {
    sX[q] = (unsigned)(rg * 128 + 16 * w) * 1024 + ((q + rot) & 3) * 256;
    sW[q] = (unsigned)(16 * w) * WS_ + (ATT ? 128 * cg : 448 * cg + ((cg & 1) ? 64 : 0)) + ((q + rot) & 3) * 128 * WS_;
  }
  const unsigned vW3 = (cg & 1) ? -128 : 384;
  const unsigned sAct = (unsigned)WS_ACT + ((rg * 4 + tg) * 144 + 14 * cg + 2 * (grp + (cg & 1))) * 1024;
  const unsigned l16 = lane * 16;

  auto issue = [&](auto I) __attribute__((always_inline)) {
    constexpr int i = decltype(I)::value;
    if constexpr (i < N1) {
      if constexpr (it_isx(i)) dma2<slot_of(i), (it_x(i) & 1) * 128u>(vXa, vXb, rX, sX[it_x(i) >> 1], c.wdst);
      else {
        constexpr int pi = it_p(i), kq = it_q(i);
        constexpr unsigned poff = ATT ? (pi == 0 ? 2048 : pi == 1 ? 1024 : 0) : (pi < 3 ? 128 * pi : 0);
        dma2<slot_of(i), poff>(vWa, vWb, rW, (!ATT && pi == 3) ? sW[kq] + vW3 : sW[kq], c.wdst);
      }
    } else if constexpr (i < N1 + S2N) issue_s2<i - N1, slot_of(i)>(c, c.wdst);
  };
  CSTAMP(14);
  v2u ep;
  asm volatile("s_load_dwordx2 %0, %1, 0x2000" : "=s"(ep) : "s"(p.ws) : "memory");
  static_assert(WS_EPOCH == 0x2000, "epoch offset");
  const unsigned srow = rg * 128 + ((ATT ? 8 : 0) + cg) * 8 + w;
  v4u xr;
  asm volatile("buffer_load_dwordx4 %0, %1, %2, %3 offen" : "=v"(xr) : "v"(l16), "s"(rX), "s"(srow * 1024) : "memory");
  {
    const unsigned far = ((3u - (unsigned)((size_t)p.x >> 7)) & 7u) * 128u;
    touch_lines((unsigned)(rg * 128 + ((ATT ? 2 : 0) + (cg & 1)) * 32 + m) * 1024u + far, p.x, (0ull - c.m1) >> 32);
  }
  {
    const unsigned fq = (3u - (unsigned)((size_t)p.wqkv >> 7)) & 7u;
    const unsigned long long mk = (unsigned)(cg >> 1) == (fq >> 1) ? (0ull - c.m1) >> 16 : 0ull;
    touch_lines((unsigned)((blockIdx.x >> 3) * 16 + (lane & 15)) * 3072u + (2u - ((lane >> 4) & 3) % 3u) * 1024u + fq * 128u, p.wqkv, mk);
  }
  static_for<3>(issue);
  asm volatile("s_barrier" ::: "memory");
  issue(IC<3>{}); issue(IC<4>{}); issue(IC<5>{});
  CSTAMP(15);
  {
    asm volatile("s_waitcnt vmcnt(12) lgkmcnt(0)" : "+v"(xr), "+s"(ep)::"memory");
    c.tag = v2u{ep.x + 1u, ep.y};
    if (threadIdx.x < 24) *(LAS v4u*)(size_t)(L_MBH + threadIdx.x * 16) = v4u{ep.x, 0u, ep.x, 0u};
    float s1 = dot2(xr.y, ONES2, dot2(xr.x, ONES2, 0.f)) + dot2(xr.w, ONES2, dot2(xr.z, ONES2, 0.f));
    float s2 = dot2(xr.y, xr.y, dot2(xr.x, xr.x, 0.f)) + dot2(xr.w, xr.w, dot2(xr.z, xr.z, 0.f));
    s1 = row_sum16(s1); s2 = row_sum16(s2);
    s1 = rows_sum(s1); s2 = rows_sum(s2);
    const float mean = s1 * (1.f / 512.f);
    const float var = fmaxf(s2 * (1.f / 512.f) - mean * mean, 0.f) + 1e-5f;
    const v4u g = {__float_as_uint(mean), c.tag.x, __float_as_uint(var), c.tag.x ^ c.tag.y ^ 0x5bd1e995u};
    asm volatile("s_mov_b64 exec, 1\n\tbuffer_store_dwordx4 %0, %1, %2, %3 offen sc1\n\ts_mov_b64 exec, -1\n\ts_nop 0"
                 ::"v"(g), "v"(l16), "s"(c.rWs), "s"((unsigned)WS_ST + srow * 16) : "memory");
  }

  v4u ph[2][2], a2[8], hb[S2N][2];
  const unsigned tra2 = tg * 4096 + (4 * hh + q4) * 128 + ((q4 >> 1) * 64) + ((lane >> 4) & 1) * 32 + (lane & 3) * 8;
  const char* hfsrc = p.ws + WS_HF + (c.rt >> 1) * 256;
  const unsigned sH = (unsigned)WS_ACT + ((2 * c.rt + grp) * 144 + 28 * tg) * 1024;
  const unsigned sO = (unsigned)WS_ACT + ((2 * c.rt + grp) * 144 + 112 + 8 * tg) * 1024;
  auto hload = [&](auto TT) __attribute__((always_inline)) {
    constexpr int t = decltype(TT)::value;
    if constexpr (t < 12) bload16x2_sc1<(t / 4) * 2 * 2048u>(hb[t][0], hb[t][1], l16, c.rWs, sH + c.r2h[t & 3] * 2048);
    else if constexpr (t < 14) bload16x2_sc1<s2_id(t) * 2048u>(hb[t][0], hb[t][1], l16, c.rWs, sH);
    else bload16x2_sc1<0u>(hb[t][0], hb[t][1], l16, c.rWs, sO + c.r2o[t - 14] * 2048);
  };
  const unsigned tagB = c.tag.x ^ c.tag.y ^ 0x5bd1e995u;

  v4u xf[32], a[4], b[4], c0, c1;
  f32x16 acc[2];
  float hv[8], tv[4];
  f32x4 cs4 = {0.f, 0.f, 0.f, 0.f};
  const unsigned sel1 = ((lane & 31) == 0 || (lane & 31) == 20) ? ONES2 : 0u;
  const v4u sel = {sel1, sel1, sel1, sel1};
  v4u mufrag = {0u, 0u, 0u, 0u};
  const unsigned tok = 32 * tg + m;
  const unsigned xa = tok * 128 + ((hh ^ ((tok >> 1) & 7)) * 16);
  const unsigned trb = (8 * hh + q4) * 128 + ((lane >> 4) & 1) * 32 + (lane & 3) * 8;
  unsigned tra = trb + ((grp ^ (q4 >> 1)) * 64);
  const unsigned csr = L_CS + grp * 1024 + lane * 16;
  const unsigned csw = hh ? (unsigned)L_KVA + m * 8 : L_CS + grp * 1024 + (tg >> 1) * 512 + m * 16 + (tg & 1) * 8;
  unsigned base = 0;

  auto mean_fix = [&](f32x16& A, int par) __attribute__((always_inline)) { A = mfma32(*(LAS v4u*)(size_t)(csr + par * 2048), mufrag, A); };
  auto RC = [&]() __attribute__((always_inline)) { const unsigned cb = base + tg * 4096; c0 = trfrag(cb, cb + 512); c1 = trfrag(cb + 2048, cb + 2560); };
  auto RB = [&](int s) __attribute__((always_inline)) { b[s] = trfrag(base + s * 2048, base + s * 2048 + 512); };
  auto RA = [&](int s) __attribute__((always_inline)) { a[s] = trfrag(base + (4 + s) * 2048, base + (4 + s) * 2048 + 512); };
  auto MU = [&](int up, int uq, int s) __attribute__((always_inline)) { acc[up & 1] = mfma32(a[s], xf[8 * uq + 4 + s], acc[up & 1]); };
  auto MR = [&](int rp, int rq, int s) __attribute__((always_inline)) { acc[rp & 1] = mfma32(b[s], xf[8 * rq + s], (rq == 0 && s == 0) ? zero16() : acc[rp & 1]); };
  auto CS = [&]() __attribute__((always_inline)) { cs4 = mfma16(sel, c0, cs4); cs4 = mfma16(sel, c1, cs4); };
  auto CSW = [&](int rp) __attribute__((always_inline)) {
    const float cs = cs4[0];
    const unsigned hi = pk2(cs, cs);
    const float lo = cs - __uint_as_float(hi << 16);
    *(LAS v2u*)(size_t)(csw + (rp & 1) * 2048) = v2u{hi, pk2(lo, lo)};
    cs4[0] = 0.f;
  };

  const unsigned sSt = (unsigned)WS_ST + (rg * 128 + tg * 32) * 16;
  v4u stg;

  static_for<4>([&](auto Q) __attribute__((always_inline)) {
    constexpr int q = decltype(Q)::value;
    SB();
    CSTAMP(16 + 2 * q);
    sync<6>();
    CSTAMP(16 + 2 * q + 1);
    auto D = [&](auto K) __attribute__((always_inline)) { SB(); issue(IC<3 * q + 6 + decltype(K)::value>{}); SB(); };
    if constexpr (q == 2) stg = bload16_sc1(m * 16, c.rWs, sSt);
    unsigned xb = xa + slot_of(3 * q); TIE(xb);
    base = slot_of(3 * q + 2) + tra; TIE(base);
#pragma unroll
    for (int s = 0; s < 4; ++s) xf[8 * q + s] = *(LAS v4u*)(size_t)(xb ^ (32 * s));
    RB(0); RB(1);
    if constexpr (q >= 1) MU(0, q - 1, 0);
    SB();
    RB(2); RB(3); RC();
    if constexpr (q >= 1) MU(0, q - 1, 1);
    D(IC<0>{});
#pragma unroll
    for (int s = 0; s < 4; ++s) xf[8 * q + 4 + s] = *(LAS v4u*)(size_t)((xb ^ (32 * s)) + SLOT);
    if constexpr (q >= 1) MU(0, q - 1, 2);
    RA(0); SB();
    if constexpr (q >= 1) MU(0, q - 1, 3);
    RA(1); D(IC<1>{});
    RA(2); RA(3); MR(0, q, 0); CS(); SB();
    MR(0, q, 1); D(IC<2>{});
    if constexpr (q == 0 && ATT) { if (w == 7) dma2<L_PHI, 0u>(l16, l16, make_rsrc(p.wphi, 16384), cg * 2048, 0u); }
    MR(0, q, 2);
    if constexpr (q == 3) CSW(0);
    MR(0, q, 3); pin(acc[0]); SB();
    if constexpr (q == 0) STAMP(1);
  });

  static_for<N1 + 1 - 12>([&](auto I) __attribute__((always_inline)) {
    constexpr int i = decltype(I)::value + 12;
    constexpr int nstd = 2 * (s1_i0(ATT, i) - 1 - (i + 1)) + ((!ATT && i == 24) ? 8 : 0);
    constexpr int nw = (!ATT && i == 19) ? 4 : (!ATT && i == 23) ? 8 : nstd;
    static_assert(nw <= nstd, "wait");
    SB();
    CSTAMP(2 * i);
    if constexpr (s1_sync(ATT, i) && !(ATT && i == 17)) sync<nw>();
    CSTAMP(2 * i + 1);
    constexpr int up = it_p(i - 1), uq = it_q(i - 1), rp = it_p(i), rq = it_q(i);
    constexpr int ei = i - 13;
    constexpr int eb = ei >= 0 ? ei / 4 : -1, eu = ei >= 0 ? ei % 4 : 0;
    constexpr bool epi = eb >= 0 && (ATT ? eb == 0 : eb < 3);
    constexpr int si = i - 14, sb = si >= 0 ? si / 4 : -1, su = si >= 0 ? si % 4 : 0;
    constexpr bool sto = !ATT && sb >= 0 && sb < 3 && (su & 1);
    constexpr int i0 = s1_i0(ATT, i), ni = s1_ni(ATT, i);
    constexpr bool own = true, ownu = true;
    if constexpr (ATT && i == 19) {
      asm volatile("s_waitcnt vmcnt(4)" ::: "memory");
      flag_store_m(0u, p.ws + WS_KF + (4 * cg + tg) * 128 + rg * 8, c.tag, (unsigned long long)(1 - grp));
    }
    if constexpr (!ATT && i == 19) flag_store_m(0u, p.ws + WS_HF + rg * 256 + cg * 8, c.tag, c.m1);
    if constexpr (!ATT && i == 23) flag_store_m(0u, p.ws + WS_HF + rg * 256 + 64 + cg * 8, c.tag, c.m1);
    if constexpr (i >= N1 - 4) {
      dma1_sc1_m(l16, hfsrc, L_MBH, c.m16);
      if constexpr (ATT && i == N1) dma1_sc1_m(l16, p.ws + WS_KF + (4 * cg + (rg >> 2)) * 128, L_MBK, c.m16 >> 8);
    }
    if constexpr (!ATT && i == 22) { WAIT_MB(L_MBH, 8, hfsrc, c.m16 >> 12); STAMP(8); }
    if constexpr (!ATT && (i == 22 || i == 23)) { hload(IC<2 * (i - 22)>{}); hload(IC<2 * (i - 22) + 1>{}); }
    if constexpr (i == 12) {
      const int n = (blockIdx.x >> 3) * 32 + m, blk = (n >> 5) & 7;
      const unsigned far = ((3u - (unsigned)((size_t)p.w2 >> 7)) & 7u) * 128u;
      touch_lines((unsigned)(448 * (n >> 8) + 32 * ((blk & 3) + (blk >> 2) * 8) + (n & 31)) * 1024u + far, p.w2, (0ull - c.m1) >> 32);
    }
    if constexpr (i >= PF_AT && i < PF_AT + 5 * PF_STEP && (i - PF_AT) % PF_STEP == 0) {
      const int n = ((i - PF_AT) / PF_STEP) * 32 + (blockIdx.x >> 3), r = n % 24;
      const char* ps = n < 96 ? p.w2 + (size_t)(448 * (n / 24) + (r < 16 ? 128 + 8 * r : 256 + 8 * r)) * 1024 : p.wo + (size_t)(n - 96) * 8192;
      touch_lines((unsigned)lane * 128u, ps, 0ull - c.m1);
    }
    if constexpr (!ATT && i == 20) tra = trb + (((cg & 1) ^ (q4 >> 1)) * 64);

    auto body = [&](auto UU, auto RR) __attribute__((always_inline)) {
      constexpr bool U = decltype(UU)::value, R = decltype(RR)::value;
      auto D = [&](auto K) __attribute__((always_inline)) {
        constexpr int k = decltype(K)::value;
        if constexpr (ni == 1 ? k == 2 : k < ni) { SB(); issue(IC<i0 + (ni == 1 ? 0 : k)>{}); }
        SB();
      };
      auto VA = [&](int k) __attribute__((always_inline)) {
        if constexpr (epi) {
          if constexpr (ATT) hv[4 * (eu & 1) + k] = fmul(acc[eb & 1][4 * eu + k], c.lna);
          else { tv[k] = silu_a(acc[eb & 1][4 * eu + k], c); if constexpr (eu == 3) { hv[4 + k] = acc[eb & 1][12 + k]; TIE(hv[4 + k]); } }
        }
      };
      auto VB = [&](int k) __attribute__((always_inline)) { if constexpr (epi && !ATT) tv[k] = __builtin_amdgcn_rcpf(fmaf(tv[k], c.lni, c.lni)); };
      auto VC = [&](int k) __attribute__((always_inline)) { if constexpr (epi && !ATT) hv[4 * (eu & 1) + k] = fmul(eu == 3 ? hv[4 + k] : acc[eb & 1][4 * eu + k], tv[k]); };
      if constexpr (R) { base = slot_of(i) + tra; TIE(base); RC(); RB(0); }
      if constexpr (i == 12) {
        v4u g = stg; TIE(g);
        for (unsigned spin = 0; !__all(g.y == c.tag.x && g.w == tagB) && spin < SPIN_CAP; ++spin) {
          g = bload16_sc1(m * 16, c.rWs, sSt);
          asm volatile("s_waitcnt vmcnt(0)" : "+v"(g)::"memory");
        }
        const float mean = __uint_as_float(g.x), var = __uint_as_float(g.z);
        c.lna = __builtin_amdgcn_rsqf(var); c.lni = var * c.lna; c.lnz = -LOG2E * c.lna;
        const float mh = __uint_as_float(pk2(-mean, 0.f) << 16);
        const unsigned mu = pk2(mh, -mean - mh);
        mufrag = v4u{mu, mu, mu, mu};
      }
      if constexpr (U) MU(up, uq, 0);
      if constexpr (sto) bstore16_sc1<(4 * sb + (su >> 1)) * 1024u>(v4u{pk2(hv[0], hv[1]), pk2(hv[2], hv[3]), pk2(hv[4], hv[5]), pk2(hv[6], hv[7])}, l16, c.rWs, sAct);
      D(IC<0>{});
      if constexpr (R) { RB(1); RB(2); }
      if constexpr (U) MU(up, uq, 1);
      VA(0); VA(1); D(IC<1>{});
      if constexpr (R) { RB(3); RA(0); }
      if constexpr (U) MU(up, uq, 2);
      VA(2); VA(3); SB();
      if constexpr (R) RA(1);
      if constexpr (U) MU(up, uq, 3);
      if constexpr (R && own) CS();
      VB(0); VB(1); SB();
      if constexpr (U && uq == 3) mean_fix(acc[up & 1], up & 1);
      if constexpr (R) { RA(2); RA(3); MR(rp, rq, 0); }
      VC(0); VC(1); SB();
      if constexpr (R) MR(rp, rq, 1);
      VB(2); VB(3); D(IC<2>{});
      if constexpr (R) { MR(rp, rq, 2); if constexpr (own && rq == 3) CSW(rp); }
      VC(2); VC(3);
      if constexpr (ATT && epi && (eu & 1)) ph[0][eu >> 1] = v4u{pk2(hv[0], hv[1]), pk2(hv[2], hv[3]), pk2(hv[4], hv[5]), pk2(hv[6], hv[7])};
      SB();
      if constexpr (R) { MR(rp, rq, 3); pin(acc[rp & 1]); }
      SB();
    };
    using T = std::true_type; using F = std::false_type;
    if constexpr (!ATT && i >= 20) {
      if (grp == 0) body(T{}, std::integral_constant<bool, (i < N1)>{});
      else body(std::integral_constant<bool, (up < 3)>{}, F{});
    } else if constexpr (ATT && i == 17) {
      if (grp == 0) {
        body(T{}, T{});
        bar();
        f32x16 kv = zero16();
        f32x4 z4 = {0.f, 0.f, 0.f, 0.f};
        const unsigned gi1 = (lane >> 4) & 1, p4 = lane & 3;
        unsigned pa0 = slot_of(13) + tg * 8192 + (8 * hh + q4) * 64 + (((4 * gi1 + p4) ^ (4 * hh + (q4 >> 1))) * 8);
        unsigned va0 = slot_of(15) + tg * 4096 + (8 * hh + q4) * 32 + ((p4 ^ (2 * hh)) * 8);
        TIE(pa0); TIE(va0);
        const unsigned pa1 = (pa0 ^ 16u) + 256;
        const unsigned va1 = (va0 ^ 8u) + 128;
#pragma unroll
        for (int h2 = 0; h2 < 2; ++h2) {
          v4u pa[4], vb[4];
#pragma unroll
          for (int s = 0; s < 4; ++s) { pa[s] = trfrag(pa0 + (4 * h2 + s) * 1024, pa1 + (4 * h2 + s) * 1024); vb[s] = trfrag(va0 + (4 * h2 + s) * 512, va1 + (4 * h2 + s) * 512); }
          SB();
#pragma unroll
          for (int s = 0; s < 4; ++s) { kv = mfma32(pa[s], vb[s], kv); z4 = mfma16(sel, pa[s], z4); }
          SB();
        }
        const unsigned sKV = (unsigned)WS_KVP + ((4 * cg + tg) * 16 + rg) * 2048, sZ = (unsigned)WS_ZP + ((4 * cg + tg) * 16 + rg) * 128;
        const unsigned vo = (hh * 16 + m) * 16, vz = lane * 4;
        asm volatile("s_nop 15\n\ts_mov_b64 exec, %7\n\t"
                     "buffer_store_dwordx4 %0, %5, %6, %8 offen sc1\n\tbuffer_store_dwordx4 %1, %5, %6, %8 offen offset:512 sc1\n\t"
                     "buffer_store_dwordx4 %2, %5, %6, %8 offen offset:1024 sc1\n\tbuffer_store_dwordx4 %3, %5, %6, %8 offen offset:1536 sc1\n\t"
                     "s_mov_b64 exec, %10\n\tbuffer_store_dword %4, %9, %6, %11 offen sc1\n\ts_mov_b64 exec, -1\n\ts_nop 0"
                     ::"v"(__builtin_shufflevector(kv, kv, 0, 1, 2, 3)), "v"(__builtin_shufflevector(kv, kv, 4, 5, 6, 7)), "v"(__builtin_shufflevector(kv, kv, 8, 9, 10, 11)),
                       "v"(__builtin_shufflevector(kv, kv, 12, 13, 14, 15)), "v"(z4[0]), "v"(vo), "s"(c.rWs), "s"(0x0000FFFF0000FFFFull), "s"(sKV), "v"(vz), "s"(0x00000000FFFFFFFFull), "s"(sZ)
                     : "memory");
      } else { bar(); body(T{}, T{}); }
    } else body(T{}, std::integral_constant<bool, (i < N1)>{});
    if constexpr (!ATT && i == N1)
      bstore16_sc1<(4 * 2 + 1) * 1024u>(v4u{pk2(hv[0], hv[1]), pk2(hv[2], hv[3]), pk2(hv[4], hv[5]), pk2(hv[6], hv[7])}, l16, c.rWs, sAct);

    if constexpr (i == N1) {
#pragma unroll
      for (int u = 0; u < 2; ++u) {
        unsigned b0 = slot_of(i + u) + tra2; TIE(b0);
        const unsigned b1 = b0 ^ 64u;
        a2[4 * u] = trfrag(b0, b0 + 1024); a2[4 * u + 1] = trfrag(b1, b1 + 1024); a2[4 * u + 2] = trfrag(b0 + 2048, b0 + 3072); a2[4 * u + 3] = trfrag(b1 + 2048, b1 + 3072);
      }
    }
    if constexpr (ATT && i == 16) {
      {
        const unsigned f = (tok >> 2) & 3;
#pragma unroll
        for (int hd = 0; hd < 2; ++hd) {
          const unsigned va = slot_of(15) + (2 * grp + hd) * 4096 + tok * 32;
          *(LAS v2u*)(size_t)(va + ((hh ^ f) * 8)) = v2u{ph[0][hd].x, ph[0][hd].y};
          *(LAS v2u*)(size_t)(va + (((2 + hh) ^ f) * 8)) = v2u{ph[0][hd].z, ph[0][hd].w};
        }
      }
      phi_block(acc[1], c, ph, [](auto) {});
      const unsigned f = (tok >> 1) & 7;
#pragma unroll
      for (int hd = 0; hd < 2; ++hd) {
        const unsigned pa = slot_of(13) + grp * SLOT + hd * 8192 + tok * 64;
        *(LAS v2u*)(size_t)(pa + (((0 + hh) ^ f) * 8)) = v2u{ph[hd][0].x, ph[hd][0].y};
        *(LAS v2u*)(size_t)(pa + (((2 + hh) ^ f) * 8)) = v2u{ph[hd][0].z, ph[hd][0].w};
        *(LAS v2u*)(size_t)(pa + (((4 + hh) ^ f) * 8)) = v2u{ph[hd][1].x, ph[hd][1].y};
        *(LAS v2u*)(size_t)(pa + (((6 + hh) ^ f) * 8)) = v2u{ph[hd][1].z, ph[hd][1].w};
      }
    }
    if constexpr (i == 12 || i == 16 || i == 20 || i == 24) STAMP(i / 4 - 1);
  });
  STAMP(6);
  CSTAMP(62); CSTAMP(63);
  const f32x16 last = acc[ATT ? 0 : 1];
  const unsigned sMine = (unsigned)WS_ACT + (rg * 4 + tg) * 144 * 1024;
  if constexpr (ATT) {
    const char* kfsrc = p.ws + WS_KF + (4 * cg + (rg >> 2)) * 128;
    asm volatile("" : "+s"(kfsrc));
    WAIT_MB(L_MBK, 16, kfsrc, c.m16 >> 8);
    STAMP(7);
    v4u part = gload16_sc1((2 * w + hh) * 2048 + m * 16, p.ws + WS_KVP + (size_t)((4 * cg + (rg >> 2)) * 16) * 2048 + (rg & 3) * 512);
    v4u zp = gload16_sc1((m >> 1) * 128 + (m & 1) * 16, p.ws + WS_ZP + (size_t)((4 * cg + (rg >> 2)) * 16) * 128 + (rg & 3) * 32);
    WAIT_MB(L_MBH, 8, hfsrc, c.m16 >> 12);
    phi_block(last, c, ph, [&](auto K) __attribute__((always_inline)) {
      if constexpr (decltype(K)::value >= 2) { SB(); hload(IC<(decltype(K)::value >= 2 ? decltype(K)::value - 2 : 0)>{}); SB(); }
      if constexpr (decltype(K)::value == 1) {
        SB();
        wait_tie<8>(part, zp);
        f32x4 s4 = __builtin_bit_cast(f32x4, part);
#pragma unroll
        for (int k = 0; k < 4; ++k) s4[k] = swap32_add(s4[k]);
        *(LAS f32x4*)(size_t)(L_KVA + w * 512 + m * 16) = s4;
        bar();
        if (w == 0) {
          f32x4 t = *(LAS f32x4*)(size_t)(L_KVA + m * 16);
#pragma unroll
          for (int r = 1; r < 8; ++r) t += *(LAS f32x4*)(size_t)(L_KVA + r * 512 + m * 16);
          f32x4 zs = __builtin_bit_cast(f32x4, zp);
#pragma unroll
          for (int k = 0; k < 4; ++k) {
            const float v = dpp_add<0x128>(dpp_add<0x124>(dpp_add<0x122>(zs[k])));
            auto r = __builtin_amdgcn_permlane16_swap(__float_as_uint(v), __float_as_uint(v), false, false);
            const float zt = __uint_as_float(r[0]) + __uint_as_float(r[1]);
            t[k] = hh ? zt : t[k];
          }
          bstore16_m_sc1(v4u{pk2(t[0], t[1]), c.tag.x, pk2(t[2], t[3]), tagB}, l16, c.rWs, (unsigned)WS_KVG + cg * 9216 + rg * 544, 0x3FFFFFFFFull);
        }
        WAIT_MB(L_MBH + 64, 8, hfsrc + 64, c.m16 >> 12);
        SB();
      }
    });
    TIE(ph[1][0]); TIE(ph[1][1]);
    v4u g[2][4];
    const unsigned ga = (m < 16 ? hh * 16 + m : 32 + hh) * 16, gs = (unsigned)WS_KVG + cg * 9216 + grp * 8 * 544;
    for (unsigned spin = 0;; ++spin) {
      granules8(g, ga, c.rWs, gs);
      bool ok = true;
#pragma unroll
      for (int k = 0; k < 8; ++k) ok = ok && g[k >> 2][k & 3].y == c.tag.x && g[k >> 2][k & 3].w == tagB;
      if (__all(ok) || spin > SPIN_CAP) break;
      __builtin_amdgcn_s_sleep(1);
    }
    STAMP(8);
    const unsigned sL = sMine + (112 + 4 * cg + 2 * grp) * 1024;
    static_for<2>([&](auto HD) __attribute__((always_inline)) {
      constexpr int hd = decltype(HD)::value;
      f32x16 o = mfma32(v4u{g[hd][0].x, g[hd][0].z, g[hd][1].x, g[hd][1].z}, ph[hd][0], zero16());
      o = mfma32(v4u{g[hd][2].x, g[hd][2].z, g[hd][3].x, g[hd][3].z}, ph[hd][1], o);
      const float r = __builtin_amdgcn_rcpf(o[8] + 1e-6f);
      bstore16_sc1<hd * 1024u>(v4u{pk2(o[0] * r, o[1] * r), pk2(o[2] * r, o[3] * r), pk2(o[4] * r, o[5] * r), pk2(o[6] * r, o[7] * r)}, l16, c.rWs, sL);
    });
  } else if (grp == 0) {
    const unsigned sL = sMine + (14 * cg + ((cg & 1) ? 0 : 12)) * 1024;
    static_for<2>([&](auto UU) __attribute__((always_inline)) {
      constexpr int u = decltype(UU)::value;
      float h8[8];
#pragma unroll
      for (int k = 0; k < 8; ++k) h8[k] = silu_b(last[8 * u + k], silu_a(last[8 * u + k], c), c);
      bstore16_sc1<u * 1024u>(v4u{pk2(h8[0], h8[1]), pk2(h8[2], h8[3]), pk2(h8[4], h8[5]), pk2(h8[6], h8[7])}, l16, c.rWs, sL);
    });
  }
  const unsigned t8 = (unsigned)(w * 8 + (lane >> 3)), c8 = lane & 7;
  const int kq = tg, th = grp, ln = lane;
  v4u eflag, xres;
  f32x16 acc0 = zero16(), acc1 = zero16();
  static_for<S2P>([&](auto JJ) __attribute__((always_inline)) {
    constexpr int J = decltype(JJ)::value;
    SB();
    CSTAMP(64 + 2 * J);
    sync_tie<s2_wait(ATT, J), (J < S2P - 1)>(hb[2 * J][0], hb[2 * J][1], hb[2 * J + 1][0], hb[2 * J + 1][1]);
    CSTAMP(64 + 2 * J + 1);
    if constexpr (J < seq_it(ATT, 14)) dma1_sc1_m(l16, hfsrc, L_MBH, c.m16);
    if constexpr (J == seq_it(ATT, 4)) { STAMP(9); WAIT_MB(L_MBH + 64, 8, hfsrc + 64, c.m16 >> 12); STAMP(10); }
    if constexpr (J == seq_it(ATT, 8) && J > 0) { STAMP(11); WAIT_MB(L_MBH + 128, 8, hfsrc + 128, c.m16 >> 12); STAMP(12); }
    if constexpr (J == seq_it(ATT, 14)) { STAMP(13); WAIT_MB(L_MBH + 192, 8, hfsrc + 192, c.m16 >> 12); STAMP(14); }
    auto REQ = [&](auto KK) __attribute__((always_inline)) {
      constexpr int code = seq_req(ATT, J, decltype(KK)::value);
      SB();
      if constexpr (code >= 100) issue_s2<(code >= 100 ? code - 100 : 0), slot_of(N1 + (code >= 100 ? code - 100 : 0))>(c, c.wdst);
      else if constexpr (code >= 0) hload(IC<(code >= 0 && code < 100 ? code : 0)>{});
      SB();
    };
    static_assert(seq_req(ATT, J, 4) < 0, "requests per iteration");
    SB();
    {
      v4u an[8];
      static_for<2>([&](auto UU) __attribute__((always_inline)) {
        constexpr int u = decltype(UU)::value;
        unsigned b0 = slot_of(N1 + 2 * J + 2 + u) + tra2; TIE(b0);
        const unsigned b1 = b0 ^ 64u;
        acc0 = mfma32(a2[4 * u], hb[2 * J + u][0], acc0);
        if constexpr (J + 1 < S2P) { an[4 * u] = trfrag(b0, b0 + 1024); an[4 * u + 1] = trfrag(b1, b1 + 1024); }
        acc1 = mfma32(a2[4 * u + 1], hb[2 * J + u][0], acc1);
        REQ(IC<2 * u>{});
        acc0 = mfma32(a2[4 * u + 2], hb[2 * J + u][1], acc0);
        if constexpr (J + 1 < S2P) { an[4 * u + 2] = trfrag(b0 + 2048, b0 + 3072); an[4 * u + 3] = trfrag(b1 + 2048, b1 + 3072); }
        acc1 = mfma32(a2[4 * u + 3], hb[2 * J + u][1], acc1);
        REQ(IC<2 * u + 1>{});
        if constexpr (J == 0 && u == 0) {
          asm volatile("s_waitcnt vmcnt(4)\n\ts_barrier" ::: "memory");
          flag_store_m(0u, p.ws + WS_HF + rg * 256 + (ATT ? 192 : 128) + cg * 8, c.tag, c.m1);
          pin(acc0); pin(acc1);
          if constexpr (seq_it(ATT, 8) == 0) WAIT_MB(L_MBH + 128, 8, hfsrc + 128, c.m16 >> 12);
          SB();
        }
      });
      if constexpr (J + 1 < S2P) {
#pragma unroll
        for (int k = 0; k < 8; ++k) a2[k] = an[k];
      }
      pin(acc0); pin(acc1);
      SB();
    }
    if constexpr (J == 2) xres = gload16(t8 * 1024 + c8 * 16, p.x + (size_t)(64 * c.rt) * 1024 + 128 * c.ct);
    if constexpr (J == 6 && !ATT) { if (blockIdx.x == 0 && w == 1) eflag = gload16_sc1((ln >> 2) * 256 + (ln & 3) * 16, p.ws + WS_HF); }
  });
  if (!ATT && blockIdx.x == 0 && w == 1) {
    asm volatile("s_waitcnt vmcnt(0)" : "+v"(eflag)::"memory");
    for (unsigned spin = 0; spin < SPIN_CAP; ++spin) {
      if (__all(eflag.x == c.tag.x && eflag.y == c.tag.y && eflag.z == c.tag.x && eflag.w == c.tag.y)) break;
      eflag = gload16_sc1((ln >> 2) * 256 + (ln & 3) * 16, p.ws + WS_HF);
      asm volatile("s_waitcnt vmcnt(0)" : "+v"(eflag)::"memory");
    }
    if (ln == 0) gstore8_sc1(0u, p.ws + WS_EPOCH, c.tag);
  }
  CSTAMP(100);
  bar();
  CSTAMP(101);
  STAMP(15);
  {
    const unsigned ra = kq * 17408 + (32 * th + m) * 272 + hh * 16;
#pragma unroll
    for (int i = 0; i < 4; ++i) {
      *(LAS f32x4*)(size_t)(ra + i * 32) = f32x4{acc0[4 * i], acc0[4 * i + 1], acc0[4 * i + 2], acc0[4 * i + 3]};
      *(LAS f32x4*)(size_t)(ra + 128 + i * 32) = f32x4{acc1[4 * i], acc1[4 * i + 1], acc1[4 * i + 2], acc1[4 * i + 3]};
    }
  }
  bar();
  {
    const unsigned ra = t8 * 272 + c8 * 32;
    f32x4 lo = (*(LAS f32x4*)(size_t)(ra) + *(LAS f32x4*)(size_t)(ra + 17408)) + (*(LAS f32x4*)(size_t)(ra + 2 * 17408) + *(LAS f32x4*)(size_t)(ra + 3 * 17408));
    f32x4 hi = (*(LAS f32x4*)(size_t)(ra + 16) + *(LAS f32x4*)(size_t)(ra + 17408 + 16)) + (*(LAS f32x4*)(size_t)(ra + 2 * 17408 + 16) + *(LAS f32x4*)(size_t)(ra + 3 * 17408 + 16));
    TIE(xres);
    const v4u o = v4u{pk2(lo[0] + __uint_as_float(xres.x << 16), lo[1] + __uint_as_float(xres.x & 0xffff0000u)),
                      pk2(lo[2] + __uint_as_float(xres.y << 16), lo[3] + __uint_as_float(xres.y & 0xffff0000u)),
                      pk2(hi[0] + __uint_as_float(xres.z << 16), hi[1] + __uint_as_float(xres.z & 0xffff0000u)),
                      pk2(hi[2] + __uint_as_float(xres.w << 16), hi[3] + __uint_as_float(xres.w & 0xffff0000u))};
    gstore16(t8 * 1024 + c8 * 16, p.out + (size_t)(64 * c.rt) * 1024 + 128 * c.ct, o);
  }
}

__global__ void __launch_bounds__(512, 2) fused(Params p) {
  extern __shared__ __attribute__((aligned(16))) char smem[];
  if ((unsigned)(size_t)smem != 0u) return;
  asm volatile("" ::"s"(p.x), "s"(p.wqkv), "s"(p.wphi), "s"(p.wo), "s"(p.w1), "s"(p.w2), "s"(p.out), "s"(p.ws));
  Ctx c;
  const int tid = threadIdx.x;
  c.lane = tid & 63;
  c.w = __builtin_amdgcn_readfirstlane(tid >> 6);
  c.tg = c.w & 3; c.grp = c.w >> 2;
  const int bid = blockIdx.x, xcd = bid & 7, idx = bid >> 3;
  const int att = idx & 1;
  c.rg = 8 * (xcd & 1) + ((idx >> 1) & 7);
  c.cg = 2 * (xcd >> 1) + (idx >> 4);
  c.rt = 4 * xcd + (idx & 1) + 2 * (idx >> 4); c.ct = (idx >> 1) & 7;
  c.wdst = (unsigned)c.w * 2048;
  c.m16 = (unsigned)__builtin_amdgcn_readfirstlane((tid >> 6) == 4 ? 0xffff : 0); c.m1 = c.m16 >> 15;
  c.vW2a = (c.lane >> 3) * 1024 + ((c.lane & 7) ^ (((c.lane >> 4) & 1) << 2)) * 16;
  c.vW2b = c.vW2a + 8192 - 1024;
  c.s2w0 = (unsigned)(448 * (c.w >> 1) + 16 * (c.w & 1)) * 1024 + 128 * c.ct;
#pragma unroll
  for (int k = 0; k < 4; ++k) {
    const unsigned r = (k + ROT2) & 3;
    c.r2h[k] = (r & 1) + (r >> 1) * 8; c.r2o[k] = r;
    c.s2w[k] = c.s2w0 + c.r2h[k] * 32768;
    c.s2o[k] = (unsigned)(128 * (c.w >> 1) + 16 * (c.w & 1)) * 1024 + 128 * c.ct + r * 32768;
  }
  c.rW2 = make_rsrc(p.w2, 1792 * 1024); c.rWo = make_rsrc(p.wo, 512 * 1024); c.rWs = make_rsrc(p.ws, (unsigned)WS_END);
  STAMP(0);
  if (att) run<true>(p, c); else run<false>(p, c);
  STAMP(16);
  CSTAMP(102);
}
}

extern "C" void kernel_launch(void* const* d_in, const int* in_sizes, int n_in, void* d_out, int out_size, void* d_ws, size_t ws_size,
                              hipStream_t stream) {
  hh::Params p;
  p.x = (const char*)d_in[0]; p.wqkv = (const char*)d_in[1]; p.wphi = (const char*)d_in[2]; p.wo = (const char*)d_in[3];
  p.w1 = (const char*)d_in[5]; p.w2 = (const char*)d_in[6]; p.out = (char*)d_out; p.ws = (char*)d_ws;
  (void)hipFuncSetAttribute((const void*)hh::fused, hipFuncAttributeMaxDynamicSharedMemorySize, hh::LDS_BYTES);
  hh::fused<<<256, 512, hh::LDS_BYTES, stream>>>(p);
}
```

```cpp
#include <hip/hip_runtime.h>
#include <hip/hip_bf16.h>

namespace hh {
#define LAS __attribute__((address_space(3)))
#define GAS __attribute__((address_space(1)))
#define DI __device__ __forceinline__
typedef unsigned v4u __attribute__((ext_vector_type(4)));
typedef unsigned v2u __attribute__((ext_vector_type(2)));
typedef float f32x2 __attribute__((ext_vector_type(2)));
typedef float f32x4 __attribute__((ext_vector_type(4)));
typedef float f32x16 __attribute__((ext_vector_type(16)));
typedef short s16x4 __attribute__((ext_vector_type(4)));
typedef __bf16 bf16x2 __attribute__((ext_vector_type(2)));
typedef __bf16 bf16x8 __attribute__((ext_vector_type(8)));

constexpr int SLOT = 16384, NSLOT = 9;
constexpr int MISC = SLOT * NSLOT;
constexpr int L_CS = MISC + 2048;
constexpr int L_KVA = L_CS + 4096;
constexpr int L_PHI = L_KVA + 4352;
constexpr int L_MBH = L_PHI + 2048;
constexpr int L_MBK = L_MBH + 256;
constexpr int LDS_BYTES = 163840;
static_assert(L_MBK + 128 <= LDS_BYTES, "lds");

constexpr size_t WS_HF = 0, WS_KF = 512, WS_EPOCH = 256;
constexpr size_t WS_ST = 16384;
constexpr size_t WS_KVP = WS_ST + 2048 * 16;
constexpr size_t WS_ZP = WS_KVP + 32 * 16 * 2048;
constexpr size_t WS_KVG = WS_ZP + 32 * 16 * 128;
constexpr size_t WS_ACT = WS_KVG + 8 * 9216;
constexpr size_t WS_END = WS_ACT + (size_t)64 * 144 * 1024;

struct Params {
  const char* x; const char* wqkv; const char* wphi; const char* wo; const char* w1; const char* w2;
  char* out; char* ws;
};

DI f32x16 mfma32(v4u a, v4u b, f32x16 c) {
  return __builtin_amdgcn_mfma_f32_32x32x16_bf16(__builtin_bit_cast(bf16x8, a), __builtin_bit_cast(bf16x8, b), c, 0, 0, 0);
}
DI f32x4 mfma16(v4u a, v4u b, f32x4 c) {
  return __builtin_amdgcn_mfma_f32_16x16x32_bf16(__builtin_bit_cast(bf16x8, a), __builtin_bit_cast(bf16x8, b), c, 0, 0, 0);
}
DI f32x16 zero16() { f32x16 z;
#pragma unroll
  for (int i = 0; i < 16; ++i) z[i] = 0.f;
  return z; }
DI unsigned pk2(float lo, float hi) { f32x2 v = {lo, hi}; return __builtin_bit_cast(unsigned, __builtin_convertvector(v, bf16x2)); }
DI unsigned pk2v(f32x2 v) { return __builtin_bit_cast(unsigned, __builtin_convertvector(v, bf16x2)); }
DI v2u trr(unsigned addr) { return __builtin_bit_cast(v2u, __builtin_amdgcn_ds_read_tr16_b64_v4i16((LAS s16x4*)(size_t)addr)); }
DI v4u trfrag(unsigned a0, unsigned a1) { v2u lo = trr(a0), hi = trr(a1); return v4u{lo.x, lo.y, hi.x, hi.y}; }
DI float dot2(unsigned a, unsigned b, float c) {
  return __builtin_amdgcn_fdot2_f32_bf16(__builtin_bit_cast(bf16x2, a), __builtin_bit_cast(bf16x2, b), c, false);
}
extern "C" __device__ float hh_fmul_legacy(float, float) __asm("llvm.amdgcn.fmul.legacy");
DI float fmul(float a, float b) { return hh_fmul_legacy(a, b); }
template <int CTRL> DI float dpp_add(float v) { return v + __uint_as_float(__builtin_amdgcn_update_dpp(0u, __float_as_uint(v), CTRL, 0xf, 0xf, false)); }
DI float row_sum16(float v) { return dpp_add<0x128>(dpp_add<0x124>(dpp_add<0x122>(dpp_add<0x121>(v)))); }
DI float swap32_add(float v) {
  auto r = __builtin_amdgcn_permlane32_swap(__float_as_uint(v), __float_as_uint(v), false, false);
  return __uint_as_float(r[0]) + __uint_as_float(r[1]);
}
DI float rows_sum(float v) {
  auto r = __builtin_amdgcn_permlane16_swap(__float_as_uint(v), __float_as_uint(v), false, false);
  return swap32_add(__uint_as_float(r[0]) + __uint_as_float(r[1]));
}
typedef __amdgpu_buffer_rsrc_t rsrc_t;
DI rsrc_t make_rsrc(const char* base, unsigned bytes) { return __builtin_amdgcn_make_buffer_rsrc((void*)base, 0, bytes, 0x00020000); }
template <unsigned LO, unsigned GO> DI void dma2(unsigned v0, unsigned v1, rsrc_t rs, unsigned sc, unsigned ld) {
  unsigned t;
  asm volatile("s_add_u32 m0, %5, %6\n\ts_add_u32 %0, %4, %7\n\tbuffer_load_dwordx4 %1, %3, %0 offen lds\n\tbuffer_load_dwordx4 %2, %3, %0 offen offset:1024 lds"
               : "=&s"(t) : "v"(v0), "v"(v1), "s"(rs), "s"(sc), "s"(ld), "i"(LO), "i"(GO) : "memory", "scc");
}
DI void dma1_sc1_m(unsigned v0, const char* s0, unsigned dst, unsigned long long mask) {
  asm volatile("s_mov_b32 m0, %2\n\ts_mov_b64 exec, %3\n\tglobal_load_lds_dwordx4 %0, %1 sc1\n\ts_mov_b64 exec, -1" ::"v"(v0), "s"(s0), "s"(dst), "s"(mask) : "memory");
}
DI void granules8(v4u (&g)[2][4], unsigned voff, rsrc_t rs, unsigned sc) {
  asm volatile("buffer_load_dwordx4 %0, %8, %9, %10 offen sc1\n\tbuffer_load_dwordx4 %1, %8, %9, %10 offen offset:544 sc1\n\t"
               "buffer_load_dwordx4 %2, %8, %9, %10 offen offset:1088 sc1\n\tbuffer_load_dwordx4 %3, %8, %9, %10 offen offset:1632 sc1\n\t"
               "buffer_load_dwordx4 %4, %8, %9, %10 offen offset:2176 sc1\n\tbuffer_load_dwordx4 %5, %8, %9, %10 offen offset:2720 sc1\n\t"
               "buffer_load_dwordx4 %6, %8, %9, %10 offen offset:3264 sc1\n\tbuffer_load_dwordx4 %7, %8, %9, %10 offen offset:3808 sc1\n\ts_waitcnt vmcnt(0)"
               : "=&v"(g[0][0]), "=&v"(g[0][1]), "=&v"(g[0][2]), "=&v"(g[0][3]), "=&v"(g[1][0]), "=&v"(g[1][1]), "=&v"(g[1][2]), "=&v"(g[1][3])
               : "v"(voff), "s"(rs), "s"(sc) : "memory");
}
DI v4u bload16_sc1(unsigned voff, rsrc_t rs, unsigned sc) {
  v4u r; asm volatile("buffer_load_dwordx4 %0, %1, %2, %3 offen sc1" : "=v"(r) : "v"(voff), "s"(rs), "s"(sc) : "memory"); return r;
}
DI void touch_lines(unsigned v0, const char* s0, unsigned long long mask) {
  asm volatile("s_mov_b32 m0, %3\n\ts_mov_b64 exec, %2\n\tglobal_load_lds_dword %0, %1\n\ts_mov_b64 exec, -1" ::"v"(v0), "s"(s0), "s"(mask), "s"((unsigned)L_KVA) : "memory");
}
DI void flag_store_m(unsigned zero, char* sbase, v2u v, unsigned long long mask) {
  asm volatile("s_mov_b64 exec, %3\n\tglobal_store_dwordx2 %0, %1, %2 sc1\n\ts_mov_b64 exec, -1\n\ts_nop 0" ::"v"(zero), "v"(v), "s"(sbase), "s"(mask) : "memory");
}
template <int N> DI void sync() { asm volatile("s_waitcnt vmcnt(%0) lgkmcnt(0)\n\ts_barrier" ::"i"(N) : "memory"); }
DI void bar() { asm volatile("s_waitcnt lgkmcnt(0)\n\ts_barrier" ::: "memory"); }
DI v4u gload16_sc1(unsigned voff, const char* sbase) {
  v4u r; asm volatile("s_nop 4\n\tglobal_load_dwordx4 %0, %1, %2 sc1" : "=v"(r) : "v"(voff), "s"(sbase) : "memory"); return r;
}
template <unsigned GO> DI void bload16x2_sc1(v4u& a, v4u& b, unsigned voff, rsrc_t rs, unsigned sc) {
  unsigned t;
  asm volatile("s_add_u32 %2, %5, %6\n\tbuffer_load_dwordx4 %0, %3, %4, %2 offen sc1\n\tbuffer_load_dwordx4 %1, %3, %4, %2 offen offset:1024 sc1"
               : "=&v"(a), "=&v"(b), "=&s"(t) : "v"(voff), "s"(rs), "s"(sc), "i"(GO) : "memory", "scc");
}
template <unsigned GO> DI void bstore16_sc1(v4u v, unsigned voff, rsrc_t rs, unsigned sc) {
  unsigned t;
  asm volatile("s_add_u32 %0, %4, %5\n\tbuffer_store_dwordx4 %1, %2, %3, %0 offen sc1\n\ts_nop 1" : "=&s"(t) : "v"(v), "v"(voff), "s"(rs), "s"(sc), "i"(GO) : "memory", "scc");
}
DI void bstore16_m_sc1(v4u v, unsigned voff, rsrc_t rs, unsigned sc, unsigned long long mask) {
  asm volatile("s_mov_b64 exec, %4\n\tbuffer_store_dwordx4 %0, %1, %2, %3 offen sc1\n\ts_mov_b64 exec, -1\n\ts_nop 0" ::"v"(v), "v"(voff), "s"(rs), "s"(sc), "s"(mask) : "memory");
}
template <int N> DI void wait_tie(v4u& a, v4u& b) { asm volatile("s_waitcnt vmcnt(%2)" : "+v"(a), "+v"(b) : "i"(N) : "memory"); }
template <int N, bool BAR> DI void sync_tie(v4u& a, v4u& b, v4u& c, v4u& d) {
  if constexpr (BAR) asm volatile("s_waitcnt vmcnt(%4) lgkmcnt(0)\n\ts_barrier" : "+v"(a), "+v"(b), "+v"(c), "+v"(d) : "i"(N) : "memory");
  else asm volatile("s_waitcnt vmcnt(%4)" : "+v"(a), "+v"(b), "+v"(c), "+v"(d) : "i"(N) : "memory");
}
DI v4u gload16(unsigned voff, const char* sbase) {
  v4u r; asm volatile("s_nop 4\n\tglobal_load_dwordx4 %0, %1, %2" : "=v"(r) : "v"(voff), "s"(sbase) : "memory"); return r;
}
DI void gstore16(unsigned voff, char* sbase, v4u v) {
  asm volatile("s_nop 4\n\tglobal_store_dwordx4 %0, %1, %2 sc1\n\ts_nop 1" ::"v"(voff), "v"(v), "s"(sbase) : "memory");
}
DI void gstore8_sc1(unsigned voff, char* sbase, v2u v) {
  asm volatile("s_nop 4\n\tglobal_store_dwordx2 %0, %1, %2 sc1\n\ts_nop 1" ::"v"(voff), "v"(v), "s"(sbase) : "memory");
}
#define TIE(x) asm volatile("" : "+v"(x))
#define SB() __builtin_amdgcn_sched_barrier(0)
DI void pin(f32x16& a) { asm volatile("" : "+v"(a)); }

constexpr float LOG2E = 1.4426950408889634f;
constexpr unsigned ONES2 = 0x3f803f80u;
constexpr unsigned SPIN_CAP = 1u << 16;

template <int N, class F> DI void static_for(F&& f) {
  if constexpr (N > 0) { static_for<N - 1>(f); f(std::integral_constant<int, N - 1>{}); }
}

DI unsigned long long mb_read(unsigned mb, int nf, int lane) { return *(volatile LAS unsigned long long*)(size_t)(mb + (unsigned)(lane & (nf - 1)) * 8); }
DI void wait_mailbox(unsigned long long f, unsigned mb, int nf, const char* src, v2u tag, int lane, unsigned long long mask) {
  const unsigned long long t = ((unsigned long long)tag.y << 32) | tag.x;
  if (__builtin_expect(__builtin_amdgcn_ballot_w64(f != t) != 0ull, 0)) {
    for (unsigned spin = 0; spin < SPIN_CAP; ++spin) {
      dma1_sc1_m((unsigned)lane * 16, src, mb, mask);
      __builtin_amdgcn_s_sleep(2);
      if (__builtin_amdgcn_ballot_w64(mb_read(mb, nf, lane) != t) == 0ull) break;
    }
  }
  asm volatile("" ::: "memory");
}
#define WAIT_MB(mb, nf, src, mask) wait_mailbox(mb_read(mb, nf, lane), mb, nf, src, c.tag, lane, mask)

DI float max3(float a, float b, float c) { float r; asm("v_max3_f32 %0, %1, %2, %3" : "=v"(r) : "v"(a), "v"(b), "v"(c)); return r; }
DI float max2(float a, float b) { float r; asm("v_max_f32 %0, %1, %2" : "=v"(r) : "v"(a), "v"(b)); return r; }
DI float max8(const f32x16& P, int o) { return max2(max3(P[o], P[o + 1], P[o + 2]), max3(P[o + 3], P[o + 4], max3(P[o + 5], P[o + 6], P[o + 7]))); }
template <bool MAX> DI void cross2(float& a, float& b) {
  auto r = __builtin_amdgcn_permlane32_swap(__float_as_uint(a), __float_as_uint(b), false, false);
  const float t = MAX ? max2(__uint_as_float(r[0]), __uint_as_float(r[1])) : __uint_as_float(r[0]) + __uint_as_float(r[1]);
  unsigned t2 = __float_as_uint(t); asm volatile("s_nop 1" : "+v"(t2));
  auto s = __builtin_amdgcn_permlane32_swap(t2, t2, false, false);
  a = __uint_as_float(s[0]); b = __uint_as_float(s[1]);
}
template <int H, class F> DI void phi_softmax(const f32x16& P, const float sc, v4u& pos, v4u& neg, F&& hook) {
  float mp = max8(P, 0), mn = max8(P, 8);
  asm volatile("s_nop 1" : "+v"(mp), "+v"(mn));
  cross2<true>(mp, mn);
  hook(std::integral_constant<int, 4 * H + 2>{});
  const f32x2 s2 = {sc, sc}, cp = {-mp * sc, -mp * sc}, cn = {-mn * sc, -mn * sc};
  f32x2 e[8];
#pragma unroll
  for (int i = 0; i < 8; ++i) {
    const f32x2 a = __builtin_elementwise_fma(f32x2{P[2 * i], P[2 * i + 1]}, s2, i < 4 ? cp : cn);
    e[i] = f32x2{__builtin_amdgcn_exp2f(a.x), __builtin_amdgcn_exp2f(a.y)};
    if (i == 3) { asm volatile("" : "+v"(e[0]), "+v"(e[1]), "+v"(e[2]), "+v"(e[3])); hook(std::integral_constant<int, 4 * H + 3>{}); }
  }
  asm volatile("" : "+v"(e[4]), "+v"(e[5]), "+v"(e[6]), "+v"(e[7]));
  hook(std::integral_constant<int, 4 * H + 4>{});
  const f32x2 p2 = (e[0] + e[1]) + (e[2] + e[3]), n2 = (e[4] + e[5]) + (e[6] + e[7]);
  float sp = p2.x + p2.y, sn = n2.x + n2.y;
  cross2<false>(sp, sn);
  hook(std::integral_constant<int, 4 * H + 5>{});
  const float rp = __builtin_amdgcn_rcpf(sp), rn = __builtin_amdgcn_rcpf(sn);
  const f32x2 rp2 = {rp, rp}, rn2 = {rn, rn};
  pos = v4u{pk2v(e[0] * rp2), pk2v(e[1] * rp2), pk2v(e[2] * rp2), pk2v(e[3] * rp2)};
  neg = v4u{pk2v(e[4] * rn2), pk2v(e[5] * rn2), pk2v(e[6] * rn2), pk2v(e[7] * rn2)};
}

constexpr bool it_isx(int i) { return i < 12 && (i % 3) != 2; }
constexpr int it_x(int i) { return 2 * (i / 3) + (i % 3); }
constexpr int it_p(int i) { return i < 12 ? 0 : (i - 12) / 4 + 1; }
constexpr int it_q(int i) { return i < 12 ? i / 3 : (i - 12) % 4; }
constexpr int n1_of(bool att) { return att ? 20 : 24; }
constexpr unsigned slot_of(int i) { return (unsigned)(i % NSLOT) * SLOT; }
constexpr int s2_id(int j) { return j < 12 ? 2 * (j / 4) + (j & 1) + ((j & 2) ? 8 : 0) : j < 14 ? j - 6 : j; }
constexpr int S2N = 18;
constexpr int s1_ni(bool att, int i) {
  if (i == 12 || i == n1_of(att)) return 2;
  if (att) return (i >= 15 && i <= 17) ? 0 : i == 18 ? 2 : i == 19 ? 3 : 1;
  return 1;
}
constexpr int s1_i0(bool att, int i) { int n = 18; for (int k = 12; k < i; ++k) n += s1_ni(att, k); return n; }
static_assert(s1_i0(true, 21) == 29 && s1_i0(false, 25) == 33, "the first nine stage-2 W items are requested in stage 1");
constexpr bool s1_sync(bool att, int i) { return (i & 1) == 0 || (att && i == 17) || (!att && (i == 19 || i == 23)); }
constexpr int S2P = 9;
struct Rq { int code, it; };
constexpr Rq SEQ_F[] = {{0, -1}, {1, -1}, {105, -1}, {2, -1}, {3, -1}, {106, -1}, {107, -1}, {108, -1},
                        {4, 0}, {5, 0}, {6, 0}, {7, 0},  {109, 1}, {110, 1}, {111, 1}, {112, 1},  {113, 2}, {8, 2}, {9, 2}, {10, 2},
                        {114, 3}, {11, 3}, {12, 3}, {13, 3},  {115, 4}, {116, 4}, {117, 4},  {14, 5}, {15, 5}, {16, 5}, {17, 5}};
constexpr Rq SEQ_A[] = {{0, -1}, {1, -1}, {2, -1}, {3, -1}, {4, -1}, {5, -1}, {6, -1}, {7, -1},
                        {109, 0}, {110, 0}, {8, 0}, {9, 0},  {111, 1}, {112, 1}, {10, 1}, {11, 1},  {113, 2}, {114, 2}, {12, 2}, {13, 2},
                        {115, 3}, {116, 3}, {14, 3}, {15, 3},  {117, 4}, {16, 4}, {17, 4}};
constexpr int seq_n(bool att) { return att ? (int)(sizeof(SEQ_A) / sizeof(Rq)) : (int)(sizeof(SEQ_F) / sizeof(Rq)); }
constexpr Rq seq_at(bool att, int k) { return att ? SEQ_A[k] : SEQ_F[k]; }
constexpr int seq_it(bool att, int code) { for (int k = 0; k < seq_n(att); ++k) if (seq_at(att, k).code == code) return seq_at(att, k).it; return -2; }
constexpr int seq_req(bool att, int J, int r) {
  for (int k = 0; k < seq_n(att); ++k) if (seq_at(att, k).it == J && r-- == 0) return seq_at(att, k).code;
  return -1;
}
constexpr int seq_after(bool att, int code, int J) {
  int n = -1;
  for (int k = 0; k < seq_n(att); ++k) {
    if (n >= 0 && seq_at(att, k).it < J) n += 2;
    if (seq_at(att, k).code == code) n = 0;
  }
  return n < 0 ? 63 : n;
}
constexpr int imin(int a, int b) { return a < b ? a : b; }
constexpr int s2_wait(bool att, int J) {
  return imin(imin(seq_after(att, 2 * J, J), seq_after(att, 2 * J + 1, J)), imin(seq_after(att, 102 + 2 * J, J), seq_after(att, 103 + 2 * J, J)));
}
template <int V> using IC = std::integral_constant<int, V>;

struct Ctx {
  int lane, w, tg, grp, rg, cg, rt, ct;
  unsigned wdst, vW2a, vW2b;
  unsigned s2w[4], s2o[4], s2w0;
  unsigned r2h[4], r2o[4];
  rsrc_t rW2, rWo, rWs;
  v2u tag;
  unsigned long long m16, m1;
  float lna, lni, lnz;
};

template <int J, unsigned LO> DI void issue_s2(const Ctx& c, unsigned ld) {
  if constexpr (J < 12) dma2<LO, (J / 4) * 2 * 32768u>(c.vW2a, c.vW2b, c.rW2, c.s2w[J & 3], ld);
  else if constexpr (J < 14) dma2<LO, s2_id(J) * 32768u>(c.vW2a, c.vW2b, c.rW2, c.s2w0, ld);
  else dma2<LO, 0u>(c.vW2a, c.vW2b, c.rWo, c.s2o[J - 14], ld);
}
DI float silu_a(float a, const Ctx& c) { return __builtin_amdgcn_exp2f(fmul(a, c.lnz)); }
DI float silu_b(float a, float t, const Ctx& c) { return fmul(a, __builtin_amdgcn_rcpf(fmaf(t, c.lni, c.lni))); }
template <class F> DI void phi_block(const f32x16& A, const Ctx& c, v4u (&ph)[2][2], F&& between) {
  const unsigned pa = L_PHI + c.grp * 1024 + (4 * (c.lane >> 5) + ((c.lane & 15) >> 2)) * 32 + (c.lane & 3) * 8;
  const unsigned sg = (c.lane & 16) ? 0x80008000u : 0u;
  f32x16 P[2];
#pragma unroll
  for (int hd = 0; hd < 2; ++hd) {
    const v4u fr = v4u{pk2(A[8 * hd], A[8 * hd + 1]), pk2(A[8 * hd + 2], A[8 * hd + 3]), pk2(A[8 * hd + 4], A[8 * hd + 5]), pk2(A[8 * hd + 6], A[8 * hd + 7])};
    const v4u aphi = trfrag(pa + hd * 512, pa + hd * 512 + 256) ^ sg;
    P[hd] = mfma32(aphi, fr, zero16());
  }
  between(IC<0>{});
  asm volatile("s_nop 15" : "+v"(P[0]), "+v"(P[1]));
  phi_softmax<0>(P[0], -c.lnz, ph[0][0], ph[0][1], between);
  between(IC<1>{});
  phi_softmax<1>(P[1], -c.lnz, ph[1][0], ph[1][1], between);
}

template <bool ATT>
DI void run(const Params& p, Ctx& c) {
  constexpr int N1 = n1_of(ATT);
  constexpr unsigned WS_ = ATT ? 3072 : 3584;
  const int lane = c.lane, w = c.w, tg = c.tg, grp = c.grp, rg = c.rg, cg = c.cg;
  const unsigned m = lane & 31, hh = lane >> 5, q4 = (lane & 15) >> 2;
  const unsigned vXa = (lane >> 3) * 1024 + (((lane & 7) ^ (lane >> 4)) * 16), vXb = (vXa ^ 64u) + 8192 - 1024;
  const unsigned vWa = (lane >> 3) * WS_ + (c.vW2a & 127u), vWb = vWa + 8 * WS_ - 1024;
  const rsrc_t rX = make_rsrc(p.x, 2048 * 1024), rW = make_rsrc(ATT ? p.wqkv : p.w1, 512 * WS_);
  const unsigned rot = (rg + (ATT ? 2 : 0) + (cg & 1)) & 3;
  unsigned sX[4], sW[4];
#pragma unroll
  for (int q = 0; q < 4; ++q) {
    sX[q] = (unsigned)(rg * 128 + 16 * w) * 1024 + ((q + rot) & 3) * 256;
    sW[q] = (unsigned)(16 * w) * WS_ + (ATT ? 128 * cg : 448 * cg + ((cg & 1) ? 64 : 0)) + ((q + rot) & 3) * 128 * WS_;
  }
  const unsigned vW3 = (cg & 1) ? -128 : 384;
  const unsigned sAct = (unsigned)WS_ACT + ((rg * 4 + tg) * 144 + 14 * cg + 2 * (grp + (cg & 1))) * 1024;
  const unsigned l16 = lane * 16;

  auto issue = [&](auto I) __attribute__((always_inline)) {
    constexpr int i = decltype(I)::value;
    if constexpr (i < N1) {
      if constexpr (it_isx(i)) dma2<slot_of(i), (it_x(i) & 1) * 128u>(vXa, vXb, rX, sX[it_x(i) >> 1], c.wdst);
      else {
        constexpr int pi = it_p(i), kq = it_q(i);
        constexpr unsigned poff = ATT ? (pi == 0 ? 2048 : pi == 1 ? 1024 : 0) : (pi < 3 ? 128 * pi : 0);
        dma2<slot_of(i), poff>(vWa, vWb, rW, (!ATT && pi == 3) ? sW[kq] + vW3 : sW[kq], c.wdst);
      }
    } else if constexpr (i < N1 + S2N) issue_s2<i - N1, slot_of(i)>(c, c.wdst);
  };
  v2u ep;
  asm volatile("s_load_dwordx2 %0, %1, 0x100" : "=s"(ep) : "s"(p.ws) : "memory");
  static_assert(WS_EPOCH == 0x100, "epoch offset");
  const unsigned srow = rg * 128 + ((ATT ? 8 : 0) + cg) * 8 + w;
  v4u xr;
  asm volatile("buffer_load_dwordx4 %0, %1, %2, %3 offen" : "=v"(xr) : "v"(l16), "s"(rX), "s"(srow * 1024) : "memory");
  {
    const unsigned far = ((3u - (unsigned)((size_t)p.x >> 7)) & 7u) * 128u;
    touch_lines((unsigned)(rg * 128 + ((ATT ? 2 : 0) + (cg & 1)) * 32 + m) * 1024u + far, p.x, (0ull - c.m1) >> 32);
  }
  {
    const unsigned fq = (3u - (unsigned)((size_t)p.wqkv >> 7)) & 7u;
    const unsigned long long mk = (unsigned)(cg >> 1) == (fq >> 1) ? (0ull - c.m1) >> 16 : 0ull;
    touch_lines((unsigned)((blockIdx.x >> 3) * 16 + (lane & 15)) * 3072u + (2u - ((lane >> 4) & 3) % 3u) * 1024u + fq * 128u, p.wqkv, mk);
  }
  static_for<3>(issue);
  asm volatile("s_barrier" ::: "memory");
  issue(IC<3>{}); issue(IC<4>{}); issue(IC<5>{});
  {
    asm volatile("s_waitcnt vmcnt(12) lgkmcnt(0)" : "+v"(xr), "+s"(ep)::"memory");
    c.tag = v2u{ep.x + 1u, ep.y};
    if (threadIdx.x < 24) *(LAS v4u*)(size_t)(L_MBH + threadIdx.x * 16) = v4u{ep.x, 0u, ep.x, 0u};
    float s1 = dot2(xr.y, ONES2, dot2(xr.x, ONES2, 0.f)) + dot2(xr.w, ONES2, dot2(xr.z, ONES2, 0.f));
    float s2 = dot2(xr.y, xr.y, dot2(xr.x, xr.x, 0.f)) + dot2(xr.w, xr.w, dot2(xr.z, xr.z, 0.f));
    s1 = row_sum16(s1); s2 = row_sum16(s2);
    s1 = rows_sum(s1); s2 = rows_sum(s2);
    const float mean = s1 * (1.f / 512.f);
    const float var = fmaxf(s2 * (1.f / 512.f) - mean * mean, 0.f) + 1e-5f;
    const v4u g = {__float_as_uint(mean), c.tag.x, __float_as_uint(var), c.tag.x ^ c.tag.y ^ 0x5bd1e995u};
    asm volatile("s_mov_b64 exec, 1\n\tbuffer_store_dwordx4 %0, %1, %2, %3 offen sc1\n\ts_mov_b64 exec, -1\n\ts_nop 0"
                 ::"v"(g), "v"(l16), "s"(c.rWs), "s"((unsigned)WS_ST + srow * 16) : "memory");
  }

  v4u ph[2][2], a2[8], hb[S2N][2];
  const unsigned tra2 = tg * 4096 + (4 * hh + q4) * 128 + ((q4 >> 1) * 64) + ((lane >> 4) & 1) * 32 + (lane & 3) * 8;
  const char* hfsrc = p.ws + WS_HF + (c.rt >> 1) * 1024;
  const unsigned sH = (unsigned)WS_ACT + ((2 * c.rt + grp) * 144 + 28 * tg) * 1024;
  const unsigned sO = (unsigned)WS_ACT + ((2 * c.rt + grp) * 144 + 112 + 8 * tg) * 1024;
  auto hload = [&](auto TT) __attribute__((always_inline)) {
    constexpr int t = decltype(TT)::value;
    if constexpr (t < 12) bload16x2_sc1<(t / 4) * 2 * 2048u>(hb[t][0], hb[t][1], l16, c.rWs, sH + c.r2h[t & 3] * 2048);
    else if constexpr (t < 14) bload16x2_sc1<s2_id(t) * 2048u>(hb[t][0], hb[t][1], l16, c.rWs, sH);
    else bload16x2_sc1<0u>(hb[t][0], hb[t][1], l16, c.rWs, sO + c.r2o[t - 14] * 2048);
  };
  const unsigned tagB = c.tag.x ^ c.tag.y ^ 0x5bd1e995u;

  v4u xf[32], a[4], b[4], c0, c1;
  f32x16 acc[2];
  float hv[8], tv[4];
  f32x4 cs4 = {0.f, 0.f, 0.f, 0.f};
  const unsigned sel1 = ((lane & 31) == 0 || (lane & 31) == 20) ? ONES2 : 0u;
  const v4u sel = {sel1, sel1, sel1, sel1};
  v4u mufrag = {0u, 0u, 0u, 0u};
  const unsigned tok = 32 * tg + m;
  const unsigned xa = tok * 128 + ((hh ^ ((tok >> 1) & 7)) * 16);
  const unsigned trb = (8 * hh + q4) * 128 + ((lane >> 4) & 1) * 32 + (lane & 3) * 8;
  unsigned tra = trb + ((grp ^ (q4 >> 1)) * 64);
  const unsigned csr = L_CS + grp * 1024 + lane * 16;
  const unsigned csw = hh ? (unsigned)L_KVA + m * 8 : L_CS + grp * 1024 + (tg >> 1) * 512 + m * 16 + (tg & 1) * 8;
  unsigned base = 0;

  auto mean_fix = [&](f32x16& A, int par) __attribute__((always_inline)) { A = mfma32(*(LAS v4u*)(size_t)(csr + par * 2048), mufrag, A); };
  auto RC = [&]() __attribute__((always_inline)) { const unsigned cb = base + tg * 4096; c0 = trfrag(cb, cb + 512); c1 = trfrag(cb + 2048, cb + 2560); };
  auto RB = [&](int s) __attribute__((always_inline)) { b[s] = trfrag(base + s * 2048, base + s * 2048 + 512); };
  auto RA = [&](int s) __attribute__((always_inline)) { a[s] = trfrag(base + (4 + s) * 2048, base + (4 + s) * 2048 + 512); };
  auto MU = [&](int up, int uq, int s) __attribute__((always_inline)) { acc[up & 1] = mfma32(a[s], xf[8 * uq + 4 + s], acc[up & 1]); };
  auto MR = [&](int rp, int rq, int s) __attribute__((always_inline)) { acc[rp & 1] = mfma32(b[s], xf[8 * rq + s], (rq == 0 && s == 0) ? zero16() : acc[rp & 1]); };
  auto CS = [&]() __attribute__((always_inline)) { cs4 = mfma16(sel, c0, cs4); cs4 = mfma16(sel, c1, cs4); };
  auto CSW = [&](int rp) __attribute__((always_inline)) {
    const float cs = cs4[0];
    const unsigned hi = pk2(cs, cs);
    const float lo = cs - __uint_as_float(hi << 16);
    *(LAS v2u*)(size_t)(csw + (rp & 1) * 2048) = v2u{hi, pk2(lo, lo)};
    cs4[0] = 0.f;
  };

  const unsigned sSt = (unsigned)WS_ST + (rg * 128 + tg * 32) * 16;
  v4u stg;

  static_for<4>([&](auto Q) __attribute__((always_inline)) {
    constexpr int q = decltype(Q)::value;
    SB();
    sync<6>();
    auto D = [&](auto K) __attribute__((always_inline)) { SB(); issue(IC<3 * q + 6 + decltype(K)::value>{}); SB(); };
    if constexpr (q == 2) stg = bload16_sc1(m * 16, c.rWs, sSt);
    unsigned xb = xa + slot_of(3 * q); TIE(xb);
    base = slot_of(3 * q + 2) + tra; TIE(base);
#pragma unroll
    for (int s = 0; s < 4; ++s) xf[8 * q + s] = *(LAS v4u*)(size_t)(xb ^ (32 * s));
    RB(0); RB(1);
    if constexpr (q >= 1) MU(0, q - 1, 0);
    SB();
    RB(2); RB(3); RC();
    if constexpr (q >= 1) MU(0, q - 1, 1);
    D(IC<0>{});
#pragma unroll
    for (int s = 0; s < 4; ++s) xf[8 * q + 4 + s] = *(LAS v4u*)(size_t)((xb ^ (32 * s)) + SLOT);
    if constexpr (q >= 1) MU(0, q - 1, 2);
    RA(0); SB();
    if constexpr (q >= 1) MU(0, q - 1, 3);
    RA(1); D(IC<1>{});
    RA(2); RA(3); MR(0, q, 0); CS(); SB();
    MR(0, q, 1); D(IC<2>{});
    if constexpr (q == 0 && ATT) { if (w == 7) dma2<L_PHI, 0u>(l16, l16, make_rsrc(p.wphi, 16384), cg * 2048, 0u); }
    MR(0, q, 2);
    if constexpr (q == 3) CSW(0);
    MR(0, q, 3); pin(acc[0]); SB();
  });

  static_for<N1 + 1 - 12>([&](auto I) __attribute__((always_inline)) {
    constexpr int i = decltype(I)::value + 12;
    constexpr int nstd = 2 * (s1_i0(ATT, i) - 1 - (i + 1)) + ((!ATT && i == 24) ? 8 : 0);
    constexpr int nw = (!ATT && i == 19) ? 4 : (!ATT && i == 23) ? 8 : nstd;
    static_assert(nw <= nstd, "wait");
    SB();
    if constexpr (s1_sync(ATT, i) && !(ATT && i == 17)) sync<nw>();
    constexpr int up = it_p(i - 1), uq = it_q(i - 1), rp = it_p(i), rq = it_q(i);
    constexpr int ei = i - 13;
    constexpr int eb = ei >= 0 ? ei / 4 : -1, eu = ei >= 0 ? ei % 4 : 0;
    constexpr bool epi = eb >= 0 && (ATT ? eb == 0 : eb < 3);
    constexpr int si = i - 14, sb = si >= 0 ? si / 4 : -1, su = si >= 0 ? si % 4 : 0;
    constexpr bool sto = !ATT && sb >= 0 && sb < 3 && (su & 1);
    constexpr int i0 = s1_i0(ATT, i), ni = s1_ni(ATT, i);
    if constexpr (ATT && i == 19) {
      asm volatile("s_waitcnt vmcnt(4)" ::: "memory");
      flag_store_m(0u, p.ws + WS_KF + (2 * cg + (tg >> 1)) * 1024 + (tg & 1) * 128 + rg * 8, c.tag, (unsigned long long)(1 - grp));
    }
    if constexpr (!ATT && i == 19) flag_store_m(0u, p.ws + WS_HF + rg * 1024 + cg * 8, c.tag, c.m1);
    if constexpr (!ATT && i == 23) flag_store_m(0u, p.ws + WS_HF + rg * 1024 + 64 + cg * 8, c.tag, c.m1);
    if constexpr (i >= N1 - 4) {
      dma1_sc1_m(l16, hfsrc, L_MBH, c.m16);
      if constexpr (ATT && i == N1) dma1_sc1_m(l16, p.ws + WS_KF + (2 * cg + (rg >> 3)) * 1024 + ((rg >> 2) & 1) * 128, L_MBK, c.m16 >> 8);
    }
    if constexpr (!ATT && i == 22) { WAIT_MB(L_MBH, 8, hfsrc, c.m16 >> 12); }
    if constexpr (!ATT && (i == 22 || i == 23)) { hload(IC<2 * (i - 22)>{}); hload(IC<2 * (i - 22) + 1>{}); }
    if constexpr (i == 12) {
      const int n = (blockIdx.x >> 3) * 32 + m, blk = (n >> 5) & 7;
      const unsigned far = ((3u - (unsigned)((size_t)p.w2 >> 7)) & 7u) * 128u;
      touch_lines((unsigned)(448 * (n >> 8) + 32 * ((blk & 3) + (blk >> 2) * 8) + (n & 31)) * 1024u + far, p.w2, (0ull - c.m1) >> 32);
    }
    if constexpr (i >= 13 && i < 18) {
      const int n = (i - 13) * 32 + (blockIdx.x >> 3), r = n % 24;
      const char* ps = n < 96 ? p.w2 + (size_t)(448 * (n / 24) + (r < 16 ? 128 + 8 * r : 256 + 8 * r)) * 1024 : p.wo + (size_t)(n - 96) * 8192;
      touch_lines((unsigned)lane * 128u, ps, 0ull - c.m1);
    }
    if constexpr (!ATT && i == 20) tra = trb + (((cg & 1) ^ (q4 >> 1)) * 64);

    auto body = [&](auto UU, auto RR) __attribute__((always_inline)) {
      constexpr bool U = decltype(UU)::value, R = decltype(RR)::value;
      auto D = [&](auto K) __attribute__((always_inline)) {
        constexpr int k = decltype(K)::value;
        if constexpr (ni == 1 ? k == 2 : k < ni) { SB(); issue(IC<i0 + (ni == 1 ? 0 : k)>{}); }
        SB();
      };
      auto VA = [&](int k) __attribute__((always_inline)) {
        if constexpr (epi) {
          if constexpr (ATT) hv[4 * (eu & 1) + k] = fmul(acc[eb & 1][4 * eu + k], c.lna);
          else { tv[k] = silu_a(acc[eb & 1][4 * eu + k], c); if constexpr (eu == 3) { hv[4 + k] = acc[eb & 1][12 + k]; TIE(hv[4 + k]); } }
        }
      };
      auto VB = [&](int k) __attribute__((always_inline)) { if constexpr (epi && !ATT) tv[k] = __builtin_amdgcn_rcpf(fmaf(tv[k], c.lni, c.lni)); };
      auto VC = [&](int k) __attribute__((always_inline)) { if constexpr (epi && !ATT) hv[4 * (eu & 1) + k] = fmul(eu == 3 ? hv[4 + k] : acc[eb & 1][4 * eu + k], tv[k]); };
      if constexpr (R) { base = slot_of(i) + tra; TIE(base); RC(); RB(0); }
      if constexpr (i == 12) {
        v4u g = stg; TIE(g);
        for (unsigned spin = 0; !__all(g.y == c.tag.x && g.w == tagB) && spin < SPIN_CAP; ++spin) {
          g = bload16_sc1(m * 16, c.rWs, sSt);
          asm volatile("s_waitcnt vmcnt(0)" : "+v"(g)::"memory");
        }
        const float mean = __uint_as_float(g.x), var = __uint_as_float(g.z);
        c.lna = __builtin_amdgcn_rsqf(var); c.lni = var * c.lna; c.lnz = -LOG2E * c.lna;
        const float mh = __uint_as_float(pk2(-mean, 0.f) << 16);
        const unsigned mu = pk2(mh, -mean - mh);
        mufrag = v4u{mu, mu, mu, mu};
      }
      if constexpr (U) MU(up, uq, 0);
      if constexpr (sto) bstore16_sc1<(4 * sb + (su >> 1)) * 1024u>(v4u{pk2(hv[0], hv[1]), pk2(hv[2], hv[3]), pk2(hv[4], hv[5]), pk2(hv[6], hv[7])}, l16, c.rWs, sAct);
      D(IC<0>{});
      if constexpr (R) { RB(1); RB(2); }
      if constexpr (U) MU(up, uq, 1);
      VA(0); VA(1); D(IC<1>{});
      if constexpr (R) { RB(3); RA(0); }
      if constexpr (U) MU(up, uq, 2);
      VA(2); VA(3); SB();
      if constexpr (R) RA(1);
      if constexpr (U) MU(up, uq, 3);
      if constexpr (R) CS();
      VB(0); VB(1); SB();
      if constexpr (U && uq == 3) mean_fix(acc[up & 1], up & 1);
      if constexpr (R) { RA(2); RA(3); MR(rp, rq, 0); }
      VC(0); VC(1); SB();
      if constexpr (R) MR(rp, rq, 1);
      VB(2); VB(3); D(IC<2>{});
      if constexpr (R) { MR(rp, rq, 2); if constexpr (rq == 3) CSW(rp); }
      VC(2); VC(3);
      if constexpr (ATT && epi && (eu & 1)) ph[0][eu >> 1] = v4u{pk2(hv[0], hv[1]), pk2(hv[2], hv[3]), pk2(hv[4], hv[5]), pk2(hv[6], hv[7])};
      SB();
      if constexpr (R) { MR(rp, rq, 3); pin(acc[rp & 1]); }
      SB();
    };
    using T = std::true_type; using F = std::false_type;
    if constexpr (!ATT && i >= 20) {
      if (grp == 0) body(T{}, std::integral_constant<bool, (i < N1)>{});
      else body(std::integral_constant<bool, (up < 3)>{}, F{});
    } else if constexpr (ATT && i == 17) {
      if (grp == 0) {
        body(T{}, T{});
        bar();
        f32x16 kv = zero16();
        f32x4 z4 = {0.f, 0.f, 0.f, 0.f};
        const unsigned gi1 = (lane >> 4) & 1, p4 = lane & 3;
        unsigned pa0 = slot_of(13) + tg * 8192 + (8 * hh + q4) * 64 + (((4 * gi1 + p4) ^ (4 * hh + (q4 >> 1))) * 8);
        unsigned va0 = slot_of(15) + tg * 4096 + (8 * hh + q4) * 32 + ((p4 ^ (2 * hh)) * 8);
        TIE(pa0); TIE(va0);
        const unsigned pa1 = (pa0 ^ 16u) + 256;
        const unsigned va1 = (va0 ^ 8u) + 128;
#pragma unroll
        for (int h2 = 0; h2 < 2; ++h2) {
          v4u pa[4], vb[4];
#pragma unroll
          for (int s = 0; s < 4; ++s) { pa[s] = trfrag(pa0 + (4 * h2 + s) * 1024, pa1 + (4 * h2 + s) * 1024); vb[s] = trfrag(va0 + (4 * h2 + s) * 512, va1 + (4 * h2 + s) * 512); }
          SB();
#pragma unroll
          for (int s = 0; s < 4; ++s) { kv = mfma32(pa[s], vb[s], kv); z4 = mfma16(sel, pa[s], z4); }
          SB();
        }
        const unsigned sKV = (unsigned)WS_KVP + ((4 * cg + tg) * 16 + rg) * 2048, sZ = (unsigned)WS_ZP + ((4 * cg + tg) * 16 + rg) * 128;
        const unsigned vo = (hh * 16 + m) * 16, vz = lane * 4;
        asm volatile("s_nop 15\n\ts_mov_b64 exec, %7\n\t"
                     "buffer_store_dwordx4 %0, %5, %6, %8 offen sc1\n\tbuffer_store_dwordx4 %1, %5, %6, %8 offen offset:512 sc1\n\t"
                     "buffer_store_dwordx4 %2, %5, %6, %8 offen offset:1024 sc1\n\tbuffer_store_dwordx4 %3, %5, %6, %8 offen offset:1536 sc1\n\t"
                     "s_mov_b64 exec, %10\n\tbuffer_store_dword %4, %9, %6, %11 offen sc1\n\ts_mov_b64 exec, -1\n\ts_nop 0"
                     ::"v"(__builtin_shufflevector(kv, kv, 0, 1, 2, 3)), "v"(__builtin_shufflevector(kv, kv, 4, 5, 6, 7)), "v"(__builtin_shufflevector(kv, kv, 8, 9, 10, 11)),
                       "v"(__builtin_shufflevector(kv, kv, 12, 13, 14, 15)), "v"(z4[0]), "v"(vo), "s"(c.rWs), "s"(0x0000FFFF0000FFFFull), "s"(sKV), "v"(vz), "s"(0x00000000FFFFFFFFull), "s"(sZ)
                     : "memory");
      } else { bar(); body(T{}, T{}); }
    } else body(T{}, std::integral_constant<bool, (i < N1)>{});
    if constexpr (!ATT && i == N1)
      bstore16_sc1<(4 * 2 + 1) * 1024u>(v4u{pk2(hv[0], hv[1]), pk2(hv[2], hv[3]), pk2(hv[4], hv[5]), pk2(hv[6], hv[7])}, l16, c.rWs, sAct);

    if constexpr (i == N1) {
#pragma unroll
      for (int u = 0; u < 2; ++u) {
        unsigned b0 = slot_of(i + u) + tra2; TIE(b0);
        const unsigned b1 = b0 ^ 64u;
        a2[4 * u] = trfrag(b0, b0 + 1024); a2[4 * u + 1] = trfrag(b1, b1 + 1024); a2[4 * u + 2] = trfrag(b0 + 2048, b0 + 3072); a2[4 * u + 3] = trfrag(b1 + 2048, b1 + 3072);
      }
    }
    if constexpr (ATT && i == 16) {
      {
        const unsigned f = (tok >> 2) & 3;
#pragma unroll
        for (int hd = 0; hd < 2; ++hd) {
          const unsigned va = slot_of(15) + (2 * grp + hd) * 4096 + tok * 32;
          *(LAS v2u*)(size_t)(va + ((hh ^ f) * 8)) = v2u{ph[0][hd].x, ph[0][hd].y};
          *(LAS v2u*)(size_t)(va + (((2 + hh) ^ f) * 8)) = v2u{ph[0][hd].z, ph[0][hd].w};
        }
      }
      phi_block(acc[1], c, ph, [](auto) {});
      const unsigned f = (tok >> 1) & 7;
#pragma unroll
      for (int hd = 0; hd < 2; ++hd) {
        const unsigned pa = slot_of(13) + grp * SLOT + hd * 8192 + tok * 64;
        *(LAS v2u*)(size_t)(pa + (((0 + hh) ^ f) * 8)) = v2u{ph[hd][0].x, ph[hd][0].y};
        *(LAS v2u*)(size_t)(pa + (((2 + hh) ^ f) * 8)) = v2u{ph[hd][0].z, ph[hd][0].w};
        *(LAS v2u*)(size_t)(pa + (((4 + hh) ^ f) * 8)) = v2u{ph[hd][1].x, ph[hd][1].y};
        *(LAS v2u*)(size_t)(pa + (((6 + hh) ^ f) * 8)) = v2u{ph[hd][1].z, ph[hd][1].w};
      }
    }
  });
  const f32x16 last = acc[ATT ? 0 : 1];
  const unsigned sMine = (unsigned)WS_ACT + (rg * 4 + tg) * 144 * 1024;
  if constexpr (ATT) {
    const char* kfsrc = p.ws + WS_KF + (2 * cg + (rg >> 3)) * 1024 + ((rg >> 2) & 1) * 128;
    asm volatile("" : "+s"(kfsrc));
    WAIT_MB(L_MBK, 16, kfsrc, c.m16 >> 8);
    v4u part = gload16_sc1((2 * w + hh) * 2048 + m * 16, p.ws + WS_KVP + (size_t)((4 * cg + (rg >> 2)) * 16) * 2048 + (rg & 3) * 512);
    v4u zp = gload16_sc1((m >> 1) * 128 + (m & 1) * 16, p.ws + WS_ZP + (size_t)((4 * cg + (rg >> 2)) * 16) * 128 + (rg & 3) * 32);
    WAIT_MB(L_MBH, 8, hfsrc, c.m16 >> 12);
    phi_block(last, c, ph, [&](auto K) __attribute__((always_inline)) {
      if constexpr (decltype(K)::value >= 2) { SB(); hload(IC<(decltype(K)::value >= 2 ? decltype(K)::value - 2 : 0)>{}); SB(); }
      if constexpr (decltype(K)::value == 1) {
        SB();
        wait_tie<8>(part, zp);
        f32x4 s4 = __builtin_bit_cast(f32x4, part);
#pragma unroll
        for (int k = 0; k < 4; ++k) s4[k] = swap32_add(s4[k]);
        *(LAS f32x4*)(size_t)(L_KVA + w * 512 + m * 16) = s4;
        bar();
        if (w == 0) {
          f32x4 t = *(LAS f32x4*)(size_t)(L_KVA + m * 16);
#pragma unroll
          for (int r = 1; r < 8; ++r) t += *(LAS f32x4*)(size_t)(L_KVA + r * 512 + m * 16);
          f32x4 zs = __builtin_bit_cast(f32x4, zp);
#pragma unroll
          for (int k = 0; k < 4; ++k) {
            const float v = dpp_add<0x128>(dpp_add<0x124>(dpp_add<0x122>(zs[k])));
            auto r = __builtin_amdgcn_permlane16_swap(__float_as_uint(v), __float_as_uint(v), false, false);
            const float zt = __uint_as_float(r[0]) + __uint_as_float(r[1]);
            t[k] = hh ? zt : t[k];
          }
          bstore16_m_sc1(v4u{pk2(t[0], t[1]), c.tag.x, pk2(t[2], t[3]), tagB}, l16, c.rWs, (unsigned)WS_KVG + cg * 9216 + rg * 544, 0x3FFFFFFFFull);
        }
        WAIT_MB(L_MBH + 64, 8, hfsrc + 64, c.m16 >> 12);
        SB();
      }
    });
    TIE(ph[1][0]); TIE(ph[1][1]);
    v4u g[2][4];
    const unsigned ga = (m < 16 ? hh * 16 + m : 32 + hh) * 16, gs = (unsigned)WS_KVG + cg * 9216 + grp * 8 * 544;
    for (unsigned spin = 0;; ++spin) {
      granules8(g, ga, c.rWs, gs);
      bool ok = true;
#pragma unroll
      for (int k = 0; k < 8; ++k) ok = ok && g[k >> 2][k & 3].y == c.tag.x && g[k >> 2][k & 3].w == tagB;
      if (__all(ok) || spin > SPIN_CAP) break;
      __builtin_amdgcn_s_sleep(1);
    }
    const unsigned sL = sMine + (112 + 4 * cg + 2 * grp) * 1024;
    static_for<2>([&](auto HD) __attribute__((always_inline)) {
      constexpr int hd = decltype(HD)::value;
      f32x16 o = mfma32(v4u{g[hd][0].x, g[hd][0].z, g[hd][1].x, g[hd][1].z}, ph[hd][0], zero16());
      o = mfma32(v4u{g[hd][2].x, g[hd][2].z, g[hd][3].x, g[hd][3].z}, ph[hd][1], o);
      const float r = __builtin_amdgcn_rcpf(o[8] + 1e-6f);
      bstore16_sc1<hd * 1024u>(v4u{pk2(o[0] * r, o[1] * r), pk2(o[2] * r, o[3] * r), pk2(o[4] * r, o[5] * r), pk2(o[6] * r, o[7] * r)}, l16, c.rWs, sL);
    });
  } else if (grp == 0) {
    const unsigned sL = sMine + (14 * cg + ((cg & 1) ? 0 : 12)) * 1024;
    static_for<2>([&](auto UU) __attribute__((always_inline)) {
      constexpr int u = decltype(UU)::value;
      float h8[8];
#pragma unroll
      for (int k = 0; k < 8; ++k) h8[k] = silu_b(last[8 * u + k], silu_a(last[8 * u + k], c), c);
      bstore16_sc1<u * 1024u>(v4u{pk2(h8[0], h8[1]), pk2(h8[2], h8[3]), pk2(h8[4], h8[5]), pk2(h8[6], h8[7])}, l16, c.rWs, sL);
    });
  }
  const unsigned t8 = (unsigned)(w * 8 + (lane >> 3)), c8 = lane & 7;
  const int kq = tg, th = grp, ln = lane;
  v4u eflag, xres;
  f32x16 acc0 = zero16(), acc1 = zero16();
  static_for<S2P>([&](auto JJ) __attribute__((always_inline)) {
    constexpr int J = decltype(JJ)::value;
    SB();
    sync_tie<s2_wait(ATT, J), true>(hb[2 * J][0], hb[2 * J][1], hb[2 * J + 1][0], hb[2 * J + 1][1]);
    if constexpr (J < seq_it(ATT, 14)) dma1_sc1_m(l16, hfsrc, L_MBH, c.m16);
    if constexpr (J == seq_it(ATT, 4)) { WAIT_MB(L_MBH + 64, 8, hfsrc + 64, c.m16 >> 12); }
    if constexpr (J == seq_it(ATT, 8) && J > 0) { WAIT_MB(L_MBH + 128, 8, hfsrc + 128, c.m16 >> 12); }
    if constexpr (J == seq_it(ATT, 14)) { WAIT_MB(L_MBH + 192, 8, hfsrc + 192, c.m16 >> 12); }
    auto REQ = [&](auto KK) __attribute__((always_inline)) {
      constexpr int code = seq_req(ATT, J, decltype(KK)::value);
      SB();
      if constexpr (code >= 100) issue_s2<(code >= 100 ? code - 100 : 0), slot_of(N1 + (code >= 100 ? code - 100 : 0))>(c, c.wdst);
      else if constexpr (code >= 0) hload(IC<(code >= 0 && code < 100 ? code : 0)>{});
      SB();
    };
    static_assert(seq_req(ATT, J, 4) < 0, "requests per iteration");
    SB();
    {
      v4u an[8];
      static_for<2>([&](auto UU) __attribute__((always_inline)) {
        constexpr int u = decltype(UU)::value;
        unsigned b0 = slot_of(N1 + 2 * J + 2 + u) + tra2; TIE(b0);
        const unsigned b1 = b0 ^ 64u;
        acc0 = mfma32(a2[4 * u], hb[2 * J + u][0], acc0);
        if constexpr (J + 1 < S2P) { an[4 * u] = trfrag(b0, b0 + 1024); an[4 * u + 1] = trfrag(b1, b1 + 1024); }
        acc1 = mfma32(a2[4 * u + 1], hb[2 * J + u][0], acc1);
        REQ(IC<2 * u>{});
        acc0 = mfma32(a2[4 * u + 2], hb[2 * J + u][1], acc0);
        if constexpr (J + 1 < S2P) { an[4 * u + 2] = trfrag(b0 + 2048, b0 + 3072); an[4 * u + 3] = trfrag(b1 + 2048, b1 + 3072); }
        acc1 = mfma32(a2[4 * u + 3], hb[2 * J + u][1], acc1);
        REQ(IC<2 * u + 1>{});
        if constexpr (J == 0 && u == 0) {
          asm volatile("s_waitcnt vmcnt(4)\n\ts_barrier" ::: "memory");
          flag_store_m(0u, p.ws + WS_HF + rg * 1024 + (ATT ? 192 : 128) + cg * 8, c.tag, c.m1);
          pin(acc0); pin(acc1);
          if constexpr (seq_it(ATT, 8) == 0) WAIT_MB(L_MBH + 128, 8, hfsrc + 128, c.m16 >> 12);
          SB();
        }
      });
      if constexpr (J + 1 < S2P) {
#pragma unroll
        for (int k = 0; k < 8; ++k) a2[k] = an[k];
      }
      pin(acc0); pin(acc1);
      SB();
    }
    if constexpr (J == 2) xres = gload16(t8 * 1024 + c8 * 16, p.x + (size_t)(64 * c.rt) * 1024 + 128 * c.ct);
    if constexpr (J == 6 && !ATT) { if (blockIdx.x == 0 && w == 1) eflag = gload16_sc1((ln >> 2) * 1024 + (ln & 3) * 16, p.ws + WS_HF); }
  });
  if (!ATT && blockIdx.x == 0 && w == 1) {
    asm volatile("s_waitcnt vmcnt(0)" : "+v"(eflag)::"memory");
    for (unsigned spin = 0; spin < SPIN_CAP; ++spin) {
      if (__all(eflag.x == c.tag.x && eflag.y == c.tag.y && eflag.z == c.tag.x && eflag.w == c.tag.y)) break;
      eflag = gload16_sc1((ln >> 2) * 1024 + (ln & 3) * 16, p.ws + WS_HF);
      asm volatile("s_waitcnt vmcnt(0)" : "+v"(eflag)::"memory");
    }
    if (ln == 0) gstore8_sc1(0u, p.ws + WS_EPOCH, c.tag);
  }
  {
    const unsigned ra = kq * 17408 + (32 * th + m) * 272 + hh * 16;
#pragma unroll
    for (int i = 0; i < 4; ++i) {
      *(LAS f32x4*)(size_t)(ra + i * 32) = f32x4{acc0[4 * i], acc0[4 * i + 1], acc0[4 * i + 2], acc0[4 * i + 3]};
      *(LAS f32x4*)(size_t)(ra + 128 + i * 32) = f32x4{acc1[4 * i], acc1[4 * i + 1], acc1[4 * i + 2], acc1[4 * i + 3]};
    }
  }
  bar();
  {
    const unsigned ra = t8 * 272 + c8 * 32;
    f32x4 lo = (*(LAS f32x4*)(size_t)(ra) + *(LAS f32x4*)(size_t)(ra + 17408)) + (*(LAS f32x4*)(size_t)(ra + 2 * 17408) + *(LAS f32x4*)(size_t)(ra + 3 * 17408));
    f32x4 hi = (*(LAS f32x4*)(size_t)(ra + 16) + *(LAS f32x4*)(size_t)(ra + 17408 + 16)) + (*(LAS f32x4*)(size_t)(ra + 2 * 17408 + 16) + *(LAS f32x4*)(size_t)(ra + 3 * 17408 + 16));
    TIE(xres);
    const v4u o = v4u{pk2(lo[0] + __uint_as_float(xres.x << 16), lo[1] + __uint_as_float(xres.x & 0xffff0000u)),
                      pk2(lo[2] + __uint_as_float(xres.y << 16), lo[3] + __uint_as_float(xres.y & 0xffff0000u)),
                      pk2(hi[0] + __uint_as_float(xres.z << 16), hi[1] + __uint_as_float(xres.z & 0xffff0000u)),
                      pk2(hi[2] + __uint_as_float(xres.w << 16), hi[3] + __uint_as_float(xres.w & 0xffff0000u))};
    gstore16(t8 * 1024 + c8 * 16, p.out + (size_t)(64 * c.rt) * 1024 + 128 * c.ct, o);
  }
}

__global__ void __launch_bounds__(512, 2) fused(Params p) {
  extern __shared__ __attribute__((aligned(16))) char smem[];
  if ((unsigned)(size_t)smem != 0u) return;
  asm volatile("" ::"s"(p.x), "s"(p.wqkv), "s"(p.wphi), "s"(p.wo), "s"(p.w1), "s"(p.w2), "s"(p.out), "s"(p.ws));
  Ctx c;
  const int tid = threadIdx.x;
  c.lane = tid & 63;
  c.w = __builtin_amdgcn_readfirstlane(tid >> 6);
  c.tg = c.w & 3; c.grp = c.w >> 2;
  const int bid = blockIdx.x, xcd = bid & 7, idx = bid >> 3;
  const int att = idx & 1;
  c.rg = 8 * (xcd & 1) + ((idx >> 1) & 7);
  c.cg = 2 * (xcd >> 1) + (idx >> 4);
  c.rt = 4 * xcd + (idx >> 3); c.ct = 2 * ((idx >> 1) & 3) + ((idx + (idx >> 3)) & 1);
  c.wdst = (unsigned)c.w * 2048;
  c.m16 = (unsigned)__builtin_amdgcn_readfirstlane((tid >> 6) == 4 ? 0xffff : 0); c.m1 = c.m16 >> 15;
  c.vW2a = (c.lane >> 3) * 1024 + ((c.lane & 7) ^ (((c.lane >> 4) & 1) << 2)) * 16;
  c.vW2b = c.vW2a + 8192 - 1024;
  c.s2w0 = (unsigned)(448 * (c.w >> 1) + 16 * (c.w & 1)) * 1024 + 128 * c.ct;
#pragma unroll
  for (int k = 0; k < 4; ++k) {
    const unsigned r = (k + c.rt + (c.ct >> 1)) & 3;
    c.r2h[k] = (r & 1) + (r >> 1) * 8; c.r2o[k] = r;
    c.s2w[k] = c.s2w0 + c.r2h[k] * 32768;
    c.s2o[k] = (unsigned)(128 * (c.w >> 1) + 16 * (c.w & 1)) * 1024 + 128 * c.ct + r * 32768;
  }
  c.rW2 = make_rsrc(p.w2, 1792 * 1024); c.rWo = make_rsrc(p.wo, 512 * 1024); c.rWs = make_rsrc(p.ws, (unsigned)WS_END);
  if (att) run<true>(p, c); else run<false>(p, c);
}
}

extern "C" void kernel_launch(void* const* d_in, const int* in_sizes, int n_in, void* d_out, int out_size, void* d_ws, size_t ws_size,
                              hipStream_t stream) {
  hh::Params p;
  p.x = (const char*)d_in[0]; p.wqkv = (const char*)d_in[1]; p.wphi = (const char*)d_in[2]; p.wo = (const char*)d_in[3];
  p.w1 = (const char*)d_in[5]; p.w2 = (const char*)d_in[6]; p.out = (char*)d_out; p.ws = (char*)d_ws;
  (void)hipFuncSetAttribute((const void*)hh::fused, hipFuncAttributeMaxDynamicSharedMemorySize, hh::LDS_BYTES);
  hh::fused<<<256, 512, hh::LDS_BYTES, stream>>>(p);
}
```
